```python
import torch

def _softmax(x, axis=-1):
    return torch.softmax(x, dim=axis)
def _gelu(x):
    return torch.nn.functional.gelu(x, approximate="tanh")
def _silu(x):
    return torch.nn.functional.silu(x)
def _sigmoid(x):
    return torch.sigmoid(x)
def _layernorm(x, axis=-1, eps=1e-5):
    m = x.mean(dim=axis, keepdim=True)
    v = x.var(dim=axis, keepdim=True, unbiased=False)
    return (x - m) / torch.sqrt(v + eps)
def _scatter_add(tgt, idx, src, axis=0):
    return tgt.clone().index_add_(axis, idx, src)
def _segment_sum(src, seg, n):
    out = torch.zeros((n,) + src.shape[1:], dtype=src.dtype)
    return out.index_add_(0, seg, src)
def _pad(x, pw):
    flat = tuple(v for pair in reversed(pw) for v in pair)
    return torch.nn.functional.pad(x, flat)

kernel_name = 'parallel_sum__gqa_block_cached__geglu_ffn__decode'

def setup_inputs():

    _w = (lambda a: (lambda t: t.to(torch.bfloat16) if t.is_floating_point() else t)(torch.as_tensor(a)))
    def _A_setup():
        B, Sq, D = 2, 1, 576
        Hq, Hkv, Sk, Dh = 32, 16, 32768, 128
        assert Hq % Hkv == 0, (Hq, Hkv)
        return {
            "x": _w((lambda *s: torch.randn(*s, dtype=torch.bfloat16) * 0.05)(B, Sq, D)),
            "k_cache": _w((lambda *s: torch.randn(*s, dtype=torch.bfloat16) * 3.2)(B, Hkv, Sk, Dh)),
            "v_cache": _w((lambda *s: torch.randn(*s, dtype=torch.bfloat16) * 1.6)(B, Hkv, Sk, Dh)),
            "wq": _w((lambda *s: torch.randn(*s, dtype=torch.bfloat16) * 3.2)(D, Hq * Dh) * (D ** -0.5)),
            "wo": _w((lambda *s: torch.randn(*s, dtype=torch.bfloat16) * 1.6)(Hq * Dh, D) * ((Hq * Dh) ** -0.5)),
        }
    _A_in = _A_setup()

    def _B_setup():
        B, S, D, F = 2, 1, 576, 4608
        return {
            "x": _w((lambda *s: torch.randn(*s, dtype=torch.bfloat16) * 0.05)(B, S, D)),
            "w_gate": _w((lambda *s: torch.randn(*s, dtype=torch.bfloat16) * 0.05)(D, F)),
            "w_up": _w((lambda *s: torch.randn(*s, dtype=torch.bfloat16) * 0.05)(D, F)),
            "w_down": _w((lambda *s: torch.randn(*s, dtype=torch.bfloat16) * 0.05)(F, D)),
        }
    _B_in = _B_setup()

    return {
        "A_x": _A_in["x"],
        "A_k_cache": _A_in["k_cache"],
        "A_v_cache": _A_in["v_cache"],
        "A_wq": _A_in["wq"],
        "A_wo": _A_in["wo"],
        "B_x": _B_in["x"],
        "B_w_gate": _B_in["w_gate"],
        "B_w_up": _B_in["w_up"],
        "B_w_down": _B_in["w_down"],
    }

def reference(A_x, A_k_cache, A_v_cache, A_wq, A_wo, B_x, B_w_gate, B_w_up, B_w_down):
    def _A_ref(x, k_cache, v_cache, wq, wo):
        B, Sq, D = x.shape
        Hq, Dh = 32, 128
        Hkv = k_cache.shape[1]
        rep = Hq // Hkv
        q = (lambda x, *d: x.permute(*d))((x @ wq).reshape(B, Sq, Hq, Dh), 0, 2, 1, 3)
        k = (lambda x, n, axis: torch.repeat_interleave(x, n, dim=axis))(k_cache, rep, axis=1)
        v = (lambda x, n, axis: torch.repeat_interleave(x, n, dim=axis))(v_cache, rep, axis=1)
        scores = torch.einsum("bhqd,bhkd->bhqk", q, k) * (Dh ** -0.5)
        p = _softmax(scores, axis=-1)
        o = torch.einsum("bhqk,bhkd->bhqd", p, v)
        return (lambda x, *d: x.permute(*d))(o, 0, 2, 1, 3).reshape(B, Sq, Hq * Dh) @ wo

    def _B_ref(x, w_gate, w_up, w_down):
        return (_gelu(x @ w_gate) * (x @ w_up)) @ w_down

    x = A_x
    xn = _layernorm(x, axis=-1)
    return x + _A_ref(xn, A_k_cache, A_v_cache, A_wq, A_wo) + _B_ref(xn, B_w_gate, B_w_up, B_w_down)
```

```cpp
#include <hip/hip_runtime.h>
#include <hip/hip_bf16.h>
#include <stdint.h>

#define MIMIC 1
typedef unsigned short u16;
typedef unsigned u32x4 __attribute__((ext_vector_type(4)));
typedef float f32x4 __attribute__((ext_vector_type(4)));
typedef __bf16 bf16x2 __attribute__((ext_vector_type(2)));

constexpr int B = 2, D = 576, HQ = 32, HKV = 16, SK = 32768, DH = 128, F = 4608;
constexpr int NSPLIT = 16;
constexpr int KEYS_PER_SPLIT = SK / NSPLIT;

__device__ __forceinline__ float bf2f(u16 v) { return __uint_as_float((unsigned)v << 16); }
__device__ __forceinline__ float rbf(float f) { return __bfloat162float(__float2bfloat16(f)); }
__device__ __forceinline__ u16 f2bf(float f) { __hip_bfloat16 h = __float2bfloat16(f); return *reinterpret_cast<u16*>(&h); }
__device__ __forceinline__ float dot2(unsigned k, unsigned q, float acc) {
  return __builtin_amdgcn_fdot2_f32_bf16(__builtin_bit_cast(bf16x2, k), __builtin_bit_cast(bf16x2, q), acc, false);
}
template <int CTRL> __device__ __forceinline__ float dpp(float x) {
  return __builtin_bit_cast(float, __builtin_amdgcn_mov_dpp(__builtin_bit_cast(int, x), CTRL, 0xf, 0xf, true));
}
constexpr int XOR1 = 0xB1, XOR2 = 0x4E, XOR7 = 0x141, XOR8 = 0x128, ROR4 = 0x124;
__device__ __forceinline__ float xrow16_max(float x) {
  auto s = __builtin_amdgcn_permlane16_swap(__float_as_uint(x), __float_as_uint(x), false, false);
  x = fmaxf(__uint_as_float(s[0]), __uint_as_float(s[1]));
  auto t = __builtin_amdgcn_permlane32_swap(__float_as_uint(x), __float_as_uint(x), false, false);
  return fmaxf(__uint_as_float(t[0]), __uint_as_float(t[1]));
}
__device__ __forceinline__ float xrow16_sum(float x) {
  auto s = __builtin_amdgcn_permlane16_swap(__float_as_uint(x), __float_as_uint(x), false, false);
  x = __uint_as_float(s[0]) + __uint_as_float(s[1]);
  auto t = __builtin_amdgcn_permlane32_swap(__float_as_uint(x), __float_as_uint(x), false, false);
  return __uint_as_float(t[0]) + __uint_as_float(t[1]);
}

__device__ void layernorm_to_lds(const u16* x, float* xn, float* red) {
  const int tid = threadIdx.x, lane = tid & 63, wave = tid >> 6;
  if (wave < 2) {
    float v[9]; float s = 0.f;
    for (int i = 0; i < 9; ++i) { v[i] = bf2f(x[wave * D + lane + 64 * i]); s += v[i]; }
    for (int o = 32; o >= 1; o >>= 1) s += __shfl_xor(s, o);
    float mean = s * (1.f / D);
    float ss = 0.f;
    for (int i = 0; i < 9; ++i) { float d = v[i] - mean; ss += d * d; }
    for (int o = 32; o >= 1; o >>= 1) ss += __shfl_xor(ss, o);
    float var = ss * (1.f / D);
    float m = rbf(mean), vr = rbf(var);
    float den = rbf(sqrtf(rbf(vr + 1e-5f)));
    for (int i = 0; i < 9; ++i) xn[wave * D + lane + 64 * i] = rbf(rbf(v[i] - m) / den);
  }
  __syncthreads();
}

__device__ __forceinline__ float gelu_tanh(float x) {
  const float k0 = 0.7978845608028654f, k1 = 0.044715f;
  float inner = k0 * (x + k1 * x * x * x);
  return 0.5f * x * (1.f + tanhf(inner));
}

__global__ void __launch_bounds__(256) k_pre(const u16* x, const u16* wq, const u16* wg, const u16* wu, u16* qout, float* hout) {
  __shared__ float xn[2 * D];
  __shared__ float red[2][4][2][32];
  __shared__ float dummy[8];
  layernorm_to_lds(x, xn, dummy);
  const int tid = threadIdx.x, lane = tid & 63, wave = tid >> 6;
  const int cq = lane & 3, r = (lane >> 2) + 16 * wave;
  const bool isq = blockIdx.x < 128;
  const int nmat = isq ? 1 : 2;
  const int col0 = isq ? blockIdx.x * 32 : (blockIdx.x - 128) * 32;
  const int N = isq ? HQ * DH : F;
  for (int mt = 0; mt < nmat; ++mt) {
    const u16* W = isq ? wq : (mt == 0 ? wg : wu);
    u32x4 w[9];
    for (int i = 0; i < 9; ++i) w[i] = *reinterpret_cast<const u32x4*>(W + (size_t)(r + 64 * i) * N + col0 + cq * 8);
    float acc[2][8];
    for (int b = 0; b < 2; ++b) for (int j = 0; j < 8; ++j) acc[b][j] = 0.f;
    for (int i = 0; i < 9; ++i) {
      float x0 = xn[r + 64 * i], x1 = xn[D + r + 64 * i];
      for (int j = 0; j < 4; ++j) {
        float lo = __uint_as_float(w[i][j] << 16), hi = __uint_as_float(w[i][j] & 0xffff0000u);
        acc[0][2 * j] += x0 * lo; acc[0][2 * j + 1] += x0 * hi;
        acc[1][2 * j] += x1 * lo; acc[1][2 * j + 1] += x1 * hi;
      }
    }
    for (int b = 0; b < 2; ++b) for (int j = 0; j < 8; ++j) {
      float v = acc[b][j];
      for (int o = 4; o <= 32; o <<= 1) v += __shfl_xor(v, o);
      if (lane < 4) red[mt][wave][b][cq * 8 + j] = v;
    }
  }
  __syncthreads();
  if (tid < 64) {
    int b = tid >> 5, c = tid & 31;
    float s0 = red[0][0][b][c] + red[0][1][b][c] + red[0][2][b][c] + red[0][3][b][c];
    if (isq) { qout[b * 4096 + col0 + c] = f2bf(s0); }
    else {
      float s1 = red[1][0][b][c] + red[1][1][b][c] + red[1][2][b][c] + red[1][3][b][c];
      float g = rbf(gelu_tanh(rbf(s0))), u = rbf(s1);
      hout[b * F + col0 + c] = rbf(g * u);
    }
  }
}

__global__ void __launch_bounds__(256) k_attn(const u16* q, const u16* kc, const u16* vc, float* pml, float* po) {
  __shared__ __attribute__((aligned(16))) float lds_p[4 * 64];
  __shared__ __attribute__((aligned(16))) float mrg[4][2][4 + DH];
  const int s = blockIdx.x, g = blockIdx.y, b = g >> 4, kvh = g & 15;
  const int tid = threadIdx.x, lane = tid & 63, wave = __builtin_amdgcn_readfirstlane(tid >> 6), r = lane >> 4, c = lane & 15;
  const float SCALE = 0.088388347648318440f, LOG2E = 1.4426950408889634f;
  u32x4 qp[2];
  for (int h = 0; h < 2; ++h) qp[h] = *reinterpret_cast<const u32x4*>(q + b * 4096 + (kvh * 2 + h) * DH + 8 * c);
  const size_t base = ((size_t)g * SK + (size_t)s * KEYS_PER_SPLIT) * DH + (size_t)r * DH + 8 * c;
  const u16* kb = kc + base; const u16* vb = vc + base;
  constexpr int NIT = KEYS_PER_SPLIT / (32 * 4);
  u32x4 Kn[8], Vn[8];
  float m = -INFINITY, l = 0.f, acc[2][8];
  for (int h = 0; h < 2; ++h) for (int j = 0; j < 8; ++j) acc[h][j] = 0.f;
#define ISSUE(it) do { size_t o_ = (size_t)((it) * 4 + wave) * 32 * DH; _Pragma("unroll") for (int t = 0; t < 8; ++t) { \
    Kn[t] = __builtin_nontemporal_load(reinterpret_cast<const u32x4*>(kb + o_ + t * 4 * DH)); \
    Vn[t] = __builtin_nontemporal_load(reinterpret_cast<const u32x4*>(vb + o_ + t * 4 * DH)); } } while (0)
  ISSUE(0);
  for (int it = 0; it < NIT; ++it) {
    u32x4 Kc[8], Vc[8];
#pragma unroll
    for (int t = 0; t < 8; ++t) { Kc[t] = Kn[t]; Vc[t] = Vn[t]; }
    if (it + 1 < NIT) ISSUE(it + 1);
    float x[16];
#pragma unroll
    for (int t = 0; t < 8; ++t)
#pragma unroll
      for (int h = 0; h < 2; ++h) { float d = 0.f;
#pragma unroll
        for (int k = 0; k < 4; ++k) d = dot2(Kc[t][k], qp[h][k], d);
        x[t * 2 + h] = d; }
#define TR_STEP(HALF, CTRL, BIT) _Pragma("unroll") for (int hh = 0; hh < (HALF); ++hh) { \
      const float lo_ = x[hh], hi_ = x[hh + (HALF)]; const float t1_ = lo_ + dpp<CTRL>(lo_), t2_ = hi_ + dpp<CTRL>(hi_); \
      x[hh] = (c & (BIT)) ? t2_ : t1_; }
    TR_STEP(8, XOR8, 8) TR_STEP(4, XOR7, 4) TR_STEP(2, XOR2, 2) TR_STEP(1, XOR1, 1)
#undef TR_STEP
    float sc = rbf(rbf(x[0]) * SCALE) * LOG2E;
    float pm = sc;
    pm = fmaxf(pm, dpp<XOR2>(pm)); pm = fmaxf(pm, dpp<ROR4>(pm)); pm = fmaxf(pm, dpp<XOR8>(pm)); pm = xrow16_max(pm);
    const float mn = fmaxf(m, pm);
    const float alpha = __builtin_amdgcn_exp2f(m - mn), p = __builtin_amdgcn_exp2f(sc - mn);
    l = fmaf(l, alpha, p); m = mn;
    const float a0 = dpp<0x00>(alpha), a1 = dpp<0x55>(alpha);
#pragma unroll
    for (int j = 0; j < 8; ++j) { acc[0][j] *= a0; acc[1][j] *= a1; }
    float* prow = lds_p + (wave * 4 + r) * 16;
    prow[c] = p;
    asm volatile("s_waitcnt lgkmcnt(0)" ::: "memory");
    f32x4 pr[4];
#pragma unroll
    for (int u = 0; u < 4; ++u) pr[u] = *reinterpret_cast<const f32x4*>(prow + 4 * u);
#pragma unroll
    for (int t = 0; t < 8; ++t) {
      float vf[8];
#pragma unroll
      for (int k = 0; k < 4; ++k) { vf[2 * k] = __uint_as_float(Vc[t][k] << 16); vf[2 * k + 1] = __uint_as_float(Vc[t][k] & 0xffff0000u); }
#pragma unroll
      for (int h = 0; h < 2; ++h) { const float pg = pr[(t * 2 + h) >> 2][(t * 2 + h) & 3];
#pragma unroll
        for (int k = 0; k < 8; ++k) acc[h][k] = fmaf(pg, vf[k], acc[h][k]); } }
  }
#undef ISSUE
  for (int h = 0; h < 2; ++h) for (int k = 0; k < 8; ++k) acc[h][k] = xrow16_sum(acc[h][k]);
  { float t = l; t += dpp<XOR2>(t); t += dpp<ROR4>(t); t += dpp<XOR8>(t); l = xrow16_sum(t); }
  if (r == 0) {
    for (int h = 0; h < 2; ++h) { for (int k = 0; k < 8; ++k) mrg[wave][h][4 + 8 * c + k] = acc[h][k];
      if (c == h) { mrg[wave][h][0] = m; mrg[wave][h][1] = l; } } }
  __syncthreads();
  {
    const int h = tid >> 7, d = tid & 127;
    float M = -INFINITY;
    for (int w = 0; w < 4; ++w) M = fmaxf(M, mrg[w][h][0]);
    float L = 0.f, o = 0.f;
    for (int w = 0; w < 4; ++w) { float wt = __builtin_amdgcn_exp2f(mrg[w][h][0] - M); L += wt * mrg[w][h][1]; o += wt * mrg[w][h][4 + d]; }
    const int row = b * HQ + kvh * 2 + h;
    po[((size_t)row * NSPLIT + s) * DH + d] = o;
    if (d == 0) { pml[(row * NSPLIT + s) * 2] = M; pml[(row * NSPLIT + s) * 2 + 1] = L; }
  }
}

__global__ void __launch_bounds__(128) k_comb(const float* pml, const float* po, float* o) {
  const int row = blockIdx.x, d = threadIdx.x;
  float M = -INFINITY;
  for (int s = 0; s < NSPLIT; ++s) M = fmaxf(M, pml[(row * NSPLIT + s) * 2]);
  float L = 0.f, a = 0.f;
  for (int s = 0; s < NSPLIT; ++s) { float w = __builtin_amdgcn_exp2f(pml[(row * NSPLIT + s) * 2] - M); L += w * pml[(row * NSPLIT + s) * 2 + 1]; a += w * po[((size_t)row * NSPLIT + s) * DH + d]; }
  o[row * DH + d] = rbf(a / L);
}

__global__ void __launch_bounds__(256) k_post(const u16* x, const float* o, const float* h, const u16* wo, const u16* wd, u16* out) {
  __shared__ float red[4][2][2][8];
  const int tid = threadIdx.x, lane = tid & 63, wave = tid >> 6, col0 = blockIdx.x * 8;
  float acc[2][2][8];
  for (int m = 0; m < 2; ++m) for (int b = 0; b < 2; ++b) for (int j = 0; j < 8; ++j) acc[m][b][j] = 0.f;
  for (int i = 0; i < 16; ++i) {
    int row = tid + 256 * i;
    u32x4 w = *reinterpret_cast<const u32x4*>(wo + (size_t)row * D + col0);
    float x0 = o[row], x1 = o[4096 + row];
    for (int j = 0; j < 4; ++j) { float lo = __uint_as_float(w[j] << 16), hi = __uint_as_float(w[j] & 0xffff0000u);
      acc[0][0][2 * j] += x0 * lo; acc[0][0][2 * j + 1] += x0 * hi; acc[0][1][2 * j] += x1 * lo; acc[0][1][2 * j + 1] += x1 * hi; }
  }
  for (int i = 0; i < 18; ++i) {
    int row = tid + 256 * i;
    u32x4 w = *reinterpret_cast<const u32x4*>(wd + (size_t)row * D + col0);
    float x0 = h[row], x1 = h[F + row];
    for (int j = 0; j < 4; ++j) { float lo = __uint_as_float(w[j] << 16), hi = __uint_as_float(w[j] & 0xffff0000u);
      acc[1][0][2 * j] += x0 * lo; acc[1][0][2 * j + 1] += x0 * hi; acc[1][1][2 * j] += x1 * lo; acc[1][1][2 * j + 1] += x1 * hi; }
  }
  for (int m = 0; m < 2; ++m) for (int b = 0; b < 2; ++b) for (int j = 0; j < 8; ++j) {
    float v = acc[m][b][j];
    for (int of = 32; of >= 1; of >>= 1) v += __shfl_xor(v, of);
    if (lane == 0) red[wave][m][b][j] = v;
  }
  __syncthreads();
  if (tid < 16) {
    int b = tid >> 3, j = tid & 7;
    float A = red[0][0][b][j] + red[1][0][b][j] + red[2][0][b][j] + red[3][0][b][j];
    float Bv = red[0][1][b][j] + red[1][1][b][j] + red[2][1][b][j] + red[3][1][b][j];
    float xv = bf2f(x[b * D + col0 + j]);
    out[b * D + col0 + j] = f2bf(rbf(xv + rbf(A)) + rbf(Bv));
  }
}

extern "C" void kernel_launch(void* const* d_in, const int* in_sizes, int n_in, void* d_out, int out_size, void* d_ws, size_t ws_size, hipStream_t stream) {
  const u16* x = (const u16*)d_in[0]; const u16* kc = (const u16*)d_in[1]; const u16* vc = (const u16*)d_in[2];
  const u16* wq = (const u16*)d_in[3]; const u16* wo = (const u16*)d_in[4];
  const u16* wg = (const u16*)d_in[6]; const u16* wu = (const u16*)d_in[7]; const u16* wd = (const u16*)d_in[8];
  char* ws = (char*)d_ws;
  u16* q = (u16*)ws;
  float* h = (float*)(ws + 65536);
  float* o = (float*)(ws + 131072);
  float* pml = (float*)(ws + 196608);
  float* po = (float*)(ws + 262144);
  k_pre<<<272, 256, 0, stream>>>(x, wq, wg, wu, q, h);
  k_attn<<<dim3(NSPLIT, 32), 256, 0, stream>>>(q, kc, vc, pml, po);
  k_comb<<<64, 128, 0, stream>>>(pml, po, o);
  k_post<<<72, 256, 0, stream>>>(x, o, h, wo, wd, (u16*)d_out);
}
```

```cpp
#include <hip/hip_runtime.h>
#include <hip/hip_bf16.h>
#include <stdint.h>

namespace fk {
typedef unsigned short u16;
typedef unsigned u32x4 __attribute__((ext_vector_type(4)));
typedef unsigned u32x2 __attribute__((ext_vector_type(2)));
typedef unsigned u32x3 __attribute__((ext_vector_type(3)));
typedef float f32x4 __attribute__((ext_vector_type(4)));
typedef __bf16 bf16x8 __attribute__((ext_vector_type(8)));
typedef __bf16 bf16x2 __attribute__((ext_vector_type(2)));
#define LAS __attribute__((address_space(3)))
#define SB() __builtin_amdgcn_sched_barrier(0)

constexpr int D = 576, SK = 32768, DH = 128, F = 4608, NQ = 4096;
#define POOL 16
constexpr int CHUNK_B = 8192;
constexpr int NSTC = 128 - POOL, NSTW = NSTC / 4;
constexpr int NTK = 8 * POOL;
constexpr int GROUP_B = SK * DH * 2;
static_assert(NSTC % 4 == 0 && NSTW >= 3, "");
constexpr int WS_EPOCH = 0, WS_CNT = 4096, WS_QP = 16384, WS_PART = WS_QP + 16 * 2 * 4 * 256 * 8, WS_ML = WS_PART + 32 * 8 * 256 * 8,
              WS_Y = WS_ML + 32 * 8 * 2 * 16, WS_END = WS_Y + 144 * 128 * 64, WS_STAMP = 4 << 20;
constexpr int AUX_NT = 2, AUX_SC1 = 16, RSRC3 = 0x00020000;
constexpr unsigned SPIN_CAP = 1u << 18;
constexpr int L_XN = 0;
constexpr int L_QRED = L_XN + 2 * D;
constexpr int L_Q = L_QRED + 6 * 128;
constexpr int L_GU = L_Q + 128;
constexpr int L_H = L_GU + 2 * 4 * 32;
constexpr int L_ACC = L_H + 64;
constexpr int L_ML = L_ACC + 4 * 256;
constexpr int L_FLAG = L_ML + 16;
constexpr int L_WORDS = L_FLAG + 16;

__device__ __forceinline__ float bflo(unsigned v) { return __uint_as_float(v << 16); }
__device__ __forceinline__ float bfhi(unsigned v) { return __uint_as_float(v & 0xffff0000u); }
__device__ __forceinline__ unsigned pkbf(float lo, float hi) { bf16x2 r = {(__bf16)lo, (__bf16)hi}; return __builtin_bit_cast(unsigned, r); }
__device__ __forceinline__ float rbf(float f) { return (float)(__bf16)f; }
template <int CTRL> __device__ __forceinline__ float dpp(float x) {
  return __builtin_bit_cast(float, __builtin_amdgcn_mov_dpp(__builtin_bit_cast(int, x), CTRL, 0xf, 0xf, true));
}
template <int CTRL> __device__ __forceinline__ unsigned dppu(unsigned x) { return (unsigned)__builtin_amdgcn_mov_dpp((int)x, CTRL, 0xf, 0xf, true); }
template <int CTRL, int BANK> __device__ __forceinline__ float dpp_bank(float old, float x) {
  return __builtin_bit_cast(float, __builtin_amdgcn_update_dpp(__builtin_bit_cast(int, old), __builtin_bit_cast(int, x), CTRL, 0xf, BANK, false));
}
constexpr int XOR1 = 0xB1, XOR2 = 0x4E, ROR4 = 0x124, ROR8 = 0x128, ROR12 = 0x12C, IDENT = 0xE4;
__device__ __forceinline__ float x16_max(float x) { auto s = __builtin_amdgcn_permlane16_swap(__float_as_uint(x), __float_as_uint(x), false, false); return fmaxf(__uint_as_float(s[0]), __uint_as_float(s[1])); }
__device__ __forceinline__ float x32_max(float x) { auto s = __builtin_amdgcn_permlane32_swap(__float_as_uint(x), __float_as_uint(x), false, false); return fmaxf(__uint_as_float(s[0]), __uint_as_float(s[1])); }
__device__ __forceinline__ float x16_sum(float x) { auto s = __builtin_amdgcn_permlane16_swap(__float_as_uint(x), __float_as_uint(x), false, false); return __uint_as_float(s[0]) + __uint_as_float(s[1]); }
__device__ __forceinline__ float x32_sum(float x) { auto s = __builtin_amdgcn_permlane32_swap(__float_as_uint(x), __float_as_uint(x), false, false); return __uint_as_float(s[0]) + __uint_as_float(s[1]); }
__device__ __forceinline__ float t32_sum(float a, float b) { auto s = __builtin_amdgcn_permlane32_swap(__float_as_uint(a), __float_as_uint(b), false, false); return __uint_as_float(s[0]) + __uint_as_float(s[1]); }
__device__ __forceinline__ float t16_sum(float a, float b) { auto s = __builtin_amdgcn_permlane16_swap(__float_as_uint(a), __float_as_uint(b), false, false); return __uint_as_float(s[0]) + __uint_as_float(s[1]); }
__device__ __forceinline__ f32x4 mfma(u32x4 a, u32x4 b, f32x4 c) {
  return __builtin_amdgcn_mfma_f32_16x16x32_bf16(__builtin_bit_cast(bf16x8, a), __builtin_bit_cast(bf16x8, b), c, 0, 0, 0);
}
__device__ __forceinline__ unsigned perm_lo(unsigned odd, unsigned even) { return __builtin_amdgcn_perm(odd, even, 0x05040100u); }
__device__ __forceinline__ unsigned perm_hi(unsigned odd, unsigned even) { return __builtin_amdgcn_perm(odd, even, 0x07060302u); }
typedef __amdgpu_buffer_rsrc_t rsrc_t;
__device__ __forceinline__ rsrc_t mkrs(const void* p, int bytes) { return __builtin_amdgcn_make_buffer_rsrc((void*)p, 0, bytes, RSRC3); }

#define ST(i) do {} while (0)
#define STDECL
#define STFLUSH() do {} while (0)

__global__ void __launch_bounds__(512, 2) fused(const u16* __restrict__ x, const u16* __restrict__ kc, const u16* __restrict__ vc, const u16* __restrict__ wq,
                                                const u16* __restrict__ wo, const u16* __restrict__ wg, const u16* __restrict__ wu, const u16* __restrict__ wd,
                                                u16* out, unsigned char* ws) {
  __shared__ __attribute__((aligned(16))) float lds[L_WORDS];
  volatile LAS unsigned* flag = (volatile LAS unsigned*)(LAS float*)(lds + L_FLAG);
  const int tid = threadIdx.x, lane = tid & 63, wave8 = __builtin_amdgcn_readfirstlane(tid >> 6), wave = wave8 & 3;
  const int g = blockIdx.x >> 3, s = blockIdx.x & 7, b = g >> 4, kvh = g & 15;
  const rsrc_t rws = mkrs(ws, WS_END);
  STDECL;
  ST(0);
  if (wave8 >= 4) {
    const rsrc_t rk = mkrs(kc + (size_t)g * (SK * DH), GROUP_B), rv = mkrs(vc + (size_t)g * (SK * DH), GROUP_B);
    const int voffK = ((lane >> 2) & 3) * 256 + (lane >> 4) * 64 + (lane & 3) * 16, voffV = lane * 16;
    u32x4 KA[8], VA[8], KB[8], VB[8];
#define LDK(dst, so) _Pragma("unroll") for (int u = 0; u < 8; ++u) { dst[u] = __builtin_amdgcn_raw_buffer_load_b128(rk, voffK + (u & 3) * 1024, (so) + (u >> 2) * 4096, AUX_NT); SB(); } SB()
#define LDV(dst, so) _Pragma("unroll") for (int u = 0; u < 8; ++u) { dst[u] = __builtin_amdgcn_raw_buffer_load_b128(rv, voffV + (u & 3) * 1024, (so) + (u >> 2) * 4096, AUX_NT); SB(); } SB()
#define SOFF(i) ((s << 20) + ((i) * 4 + wave) * CHUNK_B)
    LDK(KA, SOFF(0)); LDV(VA, SOFF(0)); LDK(KB, SOFF(1)); LDV(VB, SOFF(1));
    ST(1);
    __syncthreads();
    __syncthreads();
    const unsigned epoch = flag[0];
    ST(3);
    const int n = lane & 15;
    const u32x4 qv = *reinterpret_cast<const u32x4*>(reinterpret_cast<const u16*>(lds + L_Q) + (n & 1) * 128 + (4 * (lane >> 4) + (n >> 2)) * 8);
    const u32x4 zero4 = {0u, 0u, 0u, 0u};
    const u32x4 Q0 = (n & 2) ? zero4 : qv, Q1 = (n & 2) ? qv : zero4;
    const rsrc_t rc = mkrs(ws + WS_CNT + g * 256, 4);
    const int aoff = lane == 0 ? 0 : 64;
    f32x4 acc[8];
#pragma unroll
    for (int j = 0; j < 8; ++j) acc[j] = f32x4{0.f, 0.f, 0.f, 0.f};
    float m = -INFINITY, l = 0.f;
    int tk = 0, step = 2;
    bool vA = true, vB = true;
    constexpr float SCALE = 0.088388347648318440f, LOG2E = 1.4426950408889634f;
#define STEP(KX, VX, VALID, COND) do { \
      f32x4 dd[4]; \
      const bool cur_ = VALID; \
      if (!COND || cur_) { \
      _Pragma("unroll") for (int p = 0; p < 4; ++p) { dd[p] = mfma(KX[2 * p], Q0, f32x4{0.f, 0.f, 0.f, 0.f}); dd[p] = mfma(KX[2 * p + 1], Q1, dd[p]); } \
      } \
      SB(); \
      const unsigned t_ = live ? (unsigned)__builtin_amdgcn_readfirstlane(tk) : (unsigned)NTK; \
      const int so_ = step < NSTW ? SOFF(step) : (t_ < (unsigned)NTK ? (int)(((t_ & 7) << 20) + (NSTC + (t_ >> 3)) * CHUNK_B) : GROUP_B); \
      VALID = so_ < GROUP_B; \
      live = POOL && VALID; \
      if (POOL) tk = __builtin_amdgcn_raw_ptr_buffer_atomic_add_i32(1, rc, aoff, (step + 1 >= NSTW && VALID) ? 0 : 64, 0); \
      ++step; \
      SB(); \
      LDK(KX, so_); \
      if (!COND || cur_) { \
      float x0 = dpp<IDENT>(dd[0][0]), x1 = dpp<ROR12>(dd[0][1]), x2 = dpp<ROR8>(dd[0][2]), x3 = dpp<ROR4>(dd[0][3]); \
      x0 = dpp_bank<ROR4, 2>(x0, dd[1][0]); x1 = dpp_bank<IDENT, 2>(x1, dd[1][1]); x2 = dpp_bank<ROR12, 2>(x2, dd[1][2]); x3 = dpp_bank<ROR8, 2>(x3, dd[1][3]); \
      x0 = dpp_bank<ROR8, 4>(x0, dd[2][0]); x1 = dpp_bank<ROR4, 4>(x1, dd[2][1]); x2 = dpp_bank<IDENT, 4>(x2, dd[2][2]); x3 = dpp_bank<ROR12, 4>(x3, dd[2][3]); \
      x0 = dpp_bank<ROR12, 8>(x0, dd[3][0]); x1 = dpp_bank<ROR8, 8>(x1, dd[3][1]); x2 = dpp_bank<ROR4, 8>(x2, dd[3][2]); x3 = dpp_bank<IDENT, 8>(x3, dd[3][3]); \
      const float sc = rbf(rbf((x0 + x1) + (x2 + x3)) * SCALE); \
      if (__any(sc > m)) { \
        float pm = sc; pm = fmaxf(pm, dpp<XOR2>(pm)); pm = fmaxf(pm, dpp<ROR4>(pm)); pm = fmaxf(pm, dpp<ROR8>(pm)); pm = x16_max(pm); pm = x32_max(pm); \
        const float mn = fmaxf(m, pm), al = __builtin_amdgcn_exp2f((m - mn) * LOG2E); \
        const float al0 = dpp<0x00>(al), al1 = dpp<0x55>(al); \
        l *= al; m = mn; \
        _Pragma("unroll") for (int j = 0; j < 8; ++j) { acc[j][0] *= al0; acc[j][1] *= al1; } \
      } \
      const float pp = __builtin_amdgcn_exp2f((sc - m) * LOG2E); \
      l += pp; \
      const unsigned w_ = pkbf(pp, dpp<0x102>(pp)); \
      const u32x4 PA = {w_, dppu<0x104>(w_), dppu<0x108>(w_), dppu<0x10C>(w_)}; \
      _Pragma("unroll") for (int c = 0; c < 4; ++c) { \
        const u32x4 blo = {perm_lo(VX[1][c], VX[0][c]), perm_lo(VX[3][c], VX[2][c]), perm_lo(VX[5][c], VX[4][c]), perm_lo(VX[7][c], VX[6][c])}; \
        const u32x4 bhi = {perm_hi(VX[1][c], VX[0][c]), perm_hi(VX[3][c], VX[2][c]), perm_hi(VX[5][c], VX[4][c]), perm_hi(VX[7][c], VX[6][c])}; \
        acc[2 * c] = mfma(PA, blo, acc[2 * c]); acc[2 * c + 1] = mfma(PA, bhi, acc[2 * c + 1]); } \
      } \
      SB(); \
      LDV(VX, so_); \
    } while (0)
    bool live = POOL != 0;
    do {
      STEP(KA, VA, vA, false);
      STEP(KB, VB, vB, true);
    } while (vA);
#undef STEP
    ST(4);
    l += dpp<XOR2>(l); l += dpp<ROR4>(l); l += dpp<ROR8>(l); l = x16_sum(l); l = x32_sum(l);
    if (lane < 16) {
#pragma unroll
      for (int h = 0; h < 2; ++h) {
        *reinterpret_cast<f32x4*>(lds + L_ACC + wave * 256 + h * 128 + lane * 8) = f32x4{acc[0][h], acc[1][h], acc[2][h], acc[3][h]};
        *reinterpret_cast<f32x4*>(lds + L_ACC + wave * 256 + h * 128 + lane * 8 + 4) = f32x4{acc[4][h], acc[5][h], acc[6][h], acc[7][h]};
      }
      if (lane < 2) *reinterpret_cast<float2*>(lds + L_ML + (wave * 2 + lane) * 2) = make_float2(m, l);
    }
    asm volatile("s_waitcnt lgkmcnt(0)" ::: "memory");
    unsigned old = 0;
    if (lane == 0) old = __hip_atomic_fetch_add((LAS unsigned*)&flag[2], 1u, __ATOMIC_RELAXED, __HIP_MEMORY_SCOPE_WORKGROUP);
    old = __builtin_amdgcn_readfirstlane(old);
    if (old == 3u) {
      asm volatile("" ::: "memory");
      const int h = lane >> 5;
      float mw[4], lw[4];
#pragma unroll
      for (int w = 0; w < 4; ++w) { const float2 t = *reinterpret_cast<const float2*>(lds + L_ML + (w * 2 + h) * 2); mw[w] = t.x; lw[w] = t.y; }
      const float M = fmaxf(fmaxf(mw[0], mw[1]), fmaxf(mw[2], mw[3]));
      float L = 0.f; f32x4 o = {0.f, 0.f, 0.f, 0.f};
#pragma unroll
      for (int w = 0; w < 4; ++w) {
        const float wt = __builtin_amdgcn_exp2f((mw[w] - M) * LOG2E);
        L = fmaf(wt, lw[w], L);
        o += wt * *reinterpret_cast<const f32x4*>(lds + L_ACC + w * 256 + lane * 4);
      }
      const int po = WS_PART + ((g * 8 + s) * 256 + lane * 4) * 8;
      __builtin_amdgcn_raw_buffer_store_b128(u32x4{__float_as_uint(o[0]), epoch, __float_as_uint(o[1]), epoch}, rws, po, 0, AUX_SC1);
      __builtin_amdgcn_raw_buffer_store_b128(u32x4{__float_as_uint(o[2]), epoch, __float_as_uint(o[3]), epoch}, rws, po + 16, 0, AUX_SC1);
      if ((lane & 31) == 0) __builtin_amdgcn_raw_buffer_store_b128(u32x4{__float_as_uint(M), epoch, __float_as_uint(L), epoch}, rws, WS_ML + ((g * 8 + s) * 2 + h) * 16, 0, AUX_SC1);
      if (lane == 0) flag[3] = 1u;
      ST(5);
    }
    STFLUSH();
  } else {
    __builtin_amdgcn_s_setprio(3);
    const int hw = wave;
    const rsrc_t rwq = mkrs(wq, D * NQ * 2), rx = mkrs(x, 2 * D * 2);
    const int cb = s & 3, rq = (s >> 2) + 2 * b;
    const int qrow0 = 144 * rq + 36 * hw + (lane >> 3);
    u32x4 wqr[5];
#pragma unroll
    for (int i = 0; i < 5; ++i) wqr[i] = __builtin_amdgcn_raw_buffer_load_b128(rwq, (i == 4 && lane >= 32) ? 0x7ffffff0 : (qrow0 + 8 * i) * (NQ * 2) + (kvh * 256 + cb * 64 + (lane & 7) * 8) * 2, 0, AUX_NT);
    u32x3 xr[3];
#pragma unroll
    for (int i = 0; i < 3; ++i) xr[i] = __builtin_amdgcn_raw_buffer_load_b96(rx, lane * 36 + 12 * i, 0, 0);
    const unsigned epoch = __builtin_amdgcn_raw_buffer_load_b32(rws, WS_EPOCH, 0, AUX_SC1) + 1u;
    __builtin_amdgcn_raw_buffer_store_b32(0u, rws, (s == 0 && tid == 0) ? WS_CNT + g * 256 : 0x7ffffff0, 0, AUX_SC1);
    SB();
    ST(1);
    {
      float v[18];
#pragma unroll
      for (int i = 0; i < 3; ++i)
#pragma unroll
        for (int j = 0; j < 3; ++j) { v[6 * i + 2 * j] = bflo(xr[i][j]); v[6 * i + 2 * j + 1] = bfhi(xr[i][j]); }
      float sm = 0.f;
#pragma unroll
      for (int i = 0; i < 18; ++i) sm += v[i];
      sm += dpp<XOR1>(sm); sm += dpp<XOR2>(sm); sm += dpp<ROR4>(sm); sm += dpp<ROR8>(sm); sm = x16_sum(sm);
      const float mean = sm * (1.f / D);
      float ss = 0.f;
#pragma unroll
      for (int i = 0; i < 18; ++i) { const float d = v[i] - mean; ss = fmaf(d, d, ss); }
      ss += dpp<XOR1>(ss); ss += dpp<XOR2>(ss); ss += dpp<ROR4>(ss); ss += dpp<ROR8>(ss); ss = x16_sum(ss);
      const float mb = rbf(mean), den = rbf(sqrtf(rbf(rbf(ss * (1.f / D)) + 1e-5f)));
#pragma unroll
      for (int i = 0; i < 18; i += 2) {
        float2 o; o.x = rbf(rbf(v[i] - mb) / den); o.y = rbf(rbf(v[i + 1] - mb) / den);
        *reinterpret_cast<float2*>(lds + L_XN + lane * 18 + i) = o;
      }
      if (tid < 8) flag[lane] = lane == 0 ? epoch : 0u;
      asm volatile("s_waitcnt lgkmcnt(0)" ::: "memory");
      float a0[8], a1[8];
#pragma unroll
      for (int j = 0; j < 8; ++j) { a0[j] = 0.f; a1[j] = 0.f; }
#pragma unroll
      for (int i = 0; i < 5; ++i) {
        const int rr = i == 4 ? min(qrow0 + 32, D - 1) : qrow0 + 8 * i;
        const float x0 = lds[L_XN + rr], x1 = lds[L_XN + D + rr];
#pragma unroll
        for (int j = 0; j < 4; ++j) {
          const float lo = bflo(wqr[i][j]), hi = bfhi(wqr[i][j]);
          a0[2 * j] = fmaf(x0, lo, a0[2 * j]); a0[2 * j + 1] = fmaf(x0, hi, a0[2 * j + 1]);
          a1[2 * j] = fmaf(x1, lo, a1[2 * j]); a1[2 * j + 1] = fmaf(x1, hi, a1[2 * j + 1]);
        }
      }
      float r1[8], r2[4];
#pragma unroll
      for (int j = 0; j < 8; ++j) r1[j] = t32_sum(a0[j], a1[j]);
#pragma unroll
      for (int j = 0; j < 4; ++j) { r2[j] = t16_sum(r1[j], r1[4 + j]); r2[j] += dpp<ROR8>(r2[j]); }
      if ((lane & 8) == 0) *reinterpret_cast<f32x4*>(lds + L_QRED + hw * 128 + (lane >> 5) * 64 + (lane & 7) * 8 + ((lane >> 4) & 1) * 4) = f32x4{r2[0], r2[1], r2[2], r2[3]};
      asm volatile("s_waitcnt vmcnt(0)" ::: "memory");
    }
    __syncthreads();
    ST(2);
    {
      const int t = tid;
      if (t < 128) {
        const float q = (lds[L_QRED + t] + lds[L_QRED + 128 + t]) + (lds[L_QRED + 256 + t] + lds[L_QRED + 384 + t]);
        __builtin_amdgcn_raw_buffer_store_b64(u32x2{__float_as_uint(q), epoch}, rws, WS_QP + (((kvh * 2 + (t >> 6)) * 4 + rq) * 256 + cb * 64 + (t & 63)) * 8, 0, AUX_SC1);
      }
      u32x2 p[4];
      for (unsigned spin = 0;; ++spin) {
        asm volatile("" ::: "memory");
#pragma unroll
        for (int r = 0; r < 4; ++r) p[r] = __builtin_amdgcn_raw_buffer_load_b64(rws, WS_QP + (((kvh * 2 + b) * 4 + r) * 256 + t) * 8, 0, AUX_SC1);
        const bool ok = p[0][1] == epoch && p[1][1] == epoch && p[2][1] == epoch && p[3][1] == epoch;
        if (__all(ok) || spin > SPIN_CAP) break;
      }
      const float q = (__uint_as_float(p[0][0]) + __uint_as_float(p[1][0])) + (__uint_as_float(p[2][0]) + __uint_as_float(p[3][0]));
      reinterpret_cast<__bf16*>(lds + L_Q)[t] = (__bf16)q;
    }
    __syncthreads();
    ST(3);
    const rsrc_t rwg = mkrs((hw & 1) ? wu : wg, D * F * 2), rwd = mkrs(wd, F * D * 2), rwo = mkrs(wo, NQ * D * 2);
    const int gq = lane >> 4, c = lane & 15;
    const int colA = (hw - 1) * 192 + 8 * c, colB = (hw - 1) * 192 + 128 + 4 * c;
    f32x4 ya[8], yb[4];
#pragma unroll
    for (int j = 0; j < 8; ++j) ya[j] = f32x4{0.f, 0.f, 0.f, 0.f};
#pragma unroll
    for (int j = 0; j < 4; ++j) yb[j] = f32x4{0.f, 0.f, 0.f, 0.f};
    u32x4 wa[8]; u32x2 wb[8];
#define LDW(rs, row0) _Pragma("unroll") for (int j = 0; j < 8; ++j) { \
      wa[j] = __builtin_amdgcn_raw_buffer_load_b128(rs, ((row0) + 8 * gq + j) * (D * 2) + colA * 2, 0, AUX_NT); \
      wb[j] = __builtin_amdgcn_raw_buffer_load_b64(rs, ((row0) + 8 * gq + j) * (D * 2) + colB * 2, 0, AUX_NT); }
#define YMFMA(AF) do { \
      _Pragma("unroll") for (int cc = 0; cc < 4; ++cc) { \
        const u32x4 blo = {perm_lo(wa[1][cc], wa[0][cc]), perm_lo(wa[3][cc], wa[2][cc]), perm_lo(wa[5][cc], wa[4][cc]), perm_lo(wa[7][cc], wa[6][cc])}; \
        const u32x4 bhi = {perm_hi(wa[1][cc], wa[0][cc]), perm_hi(wa[3][cc], wa[2][cc]), perm_hi(wa[5][cc], wa[4][cc]), perm_hi(wa[7][cc], wa[6][cc])}; \
        ya[2 * cc] = mfma(AF, blo, ya[2 * cc]); ya[2 * cc + 1] = mfma(AF, bhi, ya[2 * cc + 1]); } \
      _Pragma("unroll") for (int cc = 0; cc < 2; ++cc) { \
        const u32x4 blo = {perm_lo(wb[1][cc], wb[0][cc]), perm_lo(wb[3][cc], wb[2][cc]), perm_lo(wb[5][cc], wb[4][cc]), perm_lo(wb[7][cc], wb[6][cc])}; \
        const u32x4 bhi = {perm_hi(wb[1][cc], wb[0][cc]), perm_hi(wb[3][cc], wb[2][cc]), perm_hi(wb[5][cc], wb[4][cc]), perm_hi(wb[7][cc], wb[6][cc])}; \
        yb[2 * cc] = mfma(AF, blo, yb[2 * cc]); yb[2 * cc + 1] = mfma(AF, bhi, yb[2 * cc + 1]); } } while (0)
    const int nunit = ((kvh >> 1) == s) ? 2 : 1;
#pragma unroll 1
    for (int it = 0; it < nunit; ++it) {
      const int u0 = (s * 18 + (it == 0 ? kvh : 16 + (kvh & 1))) * 32;
      const int row0 = 288 * (hw >> 1) + (lane >> 2);
      u32x4 gw[18];
#pragma unroll
      for (int i = 0; i < 18; ++i) gw[i] = __builtin_amdgcn_raw_buffer_load_b128(rwg, (row0 + 16 * i) * (F * 2) + (u0 + (lane & 3) * 8) * 2, 0, AUX_NT);
      if (hw) { LDW(rwd, u0); }
      SB();
      float a[8];
#pragma unroll
      for (int j = 0; j < 8; ++j) a[j] = 0.f;
#pragma unroll
      for (int i = 0; i < 18; ++i) {
        const float xv = lds[L_XN + b * D + row0 + 16 * i];
#pragma unroll
        for (int j = 0; j < 4; ++j) { a[2 * j] = fmaf(xv, bflo(gw[i][j]), a[2 * j]); a[2 * j + 1] = fmaf(xv, bfhi(gw[i][j]), a[2 * j + 1]); }
      }
      float r1[4], r2[2];
#pragma unroll
      for (int j = 0; j < 4; ++j) r1[j] = t32_sum(a[j], a[4 + j]);
#pragma unroll
      for (int j = 0; j < 2; ++j) { r2[j] = t16_sum(r1[j], r1[2 + j]); r2[j] += dpp<ROR4>(r2[j]); r2[j] += dpp<ROR8>(r2[j]); }
      if ((lane & 12) == 0) *reinterpret_cast<float2*>(lds + L_GU + (it * 4 + hw) * 32 + (lane & 3) * 8 + (lane >> 5) * 4 + ((lane >> 4) & 1) * 2) = make_float2(r2[0], r2[1]);
      asm volatile("s_waitcnt lgkmcnt(0)" ::: "memory");
      if (lane == 0) __hip_atomic_fetch_add((LAS unsigned*)&flag[4 + it], 1u, __ATOMIC_RELAXED, __HIP_MEMORY_SCOPE_WORKGROUP);
      if (hw) {
        for (unsigned spin = 0; flag[4 + it] < 4u && spin < (SPIN_CAP << 4); ++spin) __builtin_amdgcn_s_sleep(1);
        asm volatile("" ::: "memory");
        const int cc = lane & 31;
        const float gt = rbf(lds[L_GU + (it * 4 + 0) * 32 + cc] + lds[L_GU + (it * 4 + 2) * 32 + cc]);
        const float up = rbf(lds[L_GU + (it * 4 + 1) * 32 + cc] + lds[L_GU + (it * 4 + 3) * 32 + cc]);
        const float y2 = -2.f * 0.7978845608028654f * 1.4426950408889634f * (gt + 0.044715f * gt * gt * gt);
        const float ge = rbf(gt / (1.f + __builtin_amdgcn_exp2f(y2)));
        reinterpret_cast<__bf16*>(lds + L_H + hw * 16)[cc] = (__bf16)(ge * up);
        asm volatile("s_waitcnt lgkmcnt(0)" ::: "memory");
        const u32x4 HA = *reinterpret_cast<const u32x4*>(lds + L_H + hw * 16 + gq * 4);
        YMFMA(HA);
      }
    }
    ST(4);
    if (hw) {
      LDW(rwo, kvh * 256 + s * 32);
      for (unsigned spin = 0; flag[3] == 0u && spin < (SPIN_CAP << 4); ++spin) __builtin_amdgcn_s_sleep(4);
      ST(5);
      const int sp = (lane >> 1) & 7;
      const int po = WS_PART + ((g * 8 + sp) * 256 + s * 32 + gq * 8 + (lane & 1) * 4) * 8, mo = WS_ML + ((g * 8 + sp) * 2 + (s >> 2)) * 16;
      u32x4 p0, p1, ml;
      for (unsigned spin = 0;; ++spin) {
        asm volatile("" ::: "memory");
        p0 = __builtin_amdgcn_raw_buffer_load_b128(rws, po, 0, AUX_SC1);
        p1 = __builtin_amdgcn_raw_buffer_load_b128(rws, po + 16, 0, AUX_SC1);
        ml = __builtin_amdgcn_raw_buffer_load_b128(rws, mo, 0, AUX_SC1);
        const bool ok = p0[1] == epoch && p0[3] == epoch && p1[1] == epoch && p1[3] == epoch && ml[1] == epoch && ml[3] == epoch;
        if (__all(ok) || spin > SPIN_CAP) break;
      }
      ST(6);
      const float ms = __uint_as_float(ml[0]);
      float M = ms; M = fmaxf(M, dpp<XOR2>(M)); M = fmaxf(M, dpp<ROR4>(M)); M = fmaxf(M, dpp<ROR8>(M));
      const float wt = __builtin_amdgcn_exp2f((ms - M) * 1.4426950408889634f);
      float v[5] = {wt * __uint_as_float(p0[0]), wt * __uint_as_float(p0[2]), wt * __uint_as_float(p1[0]), wt * __uint_as_float(p1[2]), wt * __uint_as_float(ml[2])};
#pragma unroll
      for (int j = 0; j < 5; ++j) { v[j] += dpp<XOR2>(v[j]); v[j] += dpp<ROR4>(v[j]); v[j] += dpp<ROR8>(v[j]); }
      const float inv = 1.f / v[4];
      const unsigned o0 = pkbf(v[0] * inv, v[1] * inv), o1 = pkbf(v[2] * inv, v[3] * inv);
      const u32x4 OA = {dppu<0xA0>(o0), dppu<0xA0>(o1), dppu<0xF5>(o0), dppu<0xF5>(o1)};
      YMFMA(OA);
      const float e0 = gq == 0 ? ya[0][0] : gq == 1 ? ya[2][0] : gq == 2 ? ya[4][0] : ya[6][0];
      const float e1 = gq == 0 ? ya[1][0] : gq == 1 ? ya[3][0] : gq == 2 ? ya[5][0] : ya[7][0];
      const float e2 = gq == 0 ? yb[0][0] : gq == 1 ? yb[1][0] : gq == 2 ? yb[2][0] : yb[3][0];
      const int prod = kvh * 8 + s;
      __builtin_amdgcn_raw_buffer_store_b128(u32x4{__float_as_uint(e0), epoch, __float_as_uint(e1), epoch}, rws,
                                             WS_Y + ((b * 72 + (colA >> 3)) * 128 + prod) * 64 + gq * 16, 0, AUX_SC1);
      __builtin_amdgcn_raw_buffer_store_b64(u32x2{__float_as_uint(e2), epoch}, rws,
                                            WS_Y + ((b * 72 + (colB >> 3)) * 128 + prod) * 64 + ((colB & 7) + gq) * 8, 0, AUX_SC1);
      ST(7);
    } else if (blockIdx.x < 144) {
      const int rid = blockIdx.x;
      const unsigned xres = __builtin_amdgcn_raw_buffer_load_b32(rx, rid * 16 + (lane & 3) * 4, 0, 0);
      for (unsigned spin = 0; flag[3] == 0u && spin < (SPIN_CAP << 4); ++spin) __builtin_amdgcn_s_sleep(4);
      ST(5);
      u32x4 y[8];
      for (unsigned spin = 0;; ++spin) {
        asm volatile("" ::: "memory");
        bool ok = true;
#pragma unroll
        for (int i = 0; i < 8; ++i) y[i] = __builtin_amdgcn_raw_buffer_load_b128(rws, WS_Y + rid * 8192 + i * 1024 + lane * 16, 0, AUX_SC1);
#pragma unroll
        for (int i = 0; i < 8; ++i) ok = ok && y[i][1] == epoch && y[i][3] == epoch;
        if (__all(ok) || spin > SPIN_CAP) break;
      }
      ST(6);
      float s0 = 0.f, s1 = 0.f;
#pragma unroll
      for (int i = 0; i < 8; ++i) { s0 += __uint_as_float(y[i][0]); s1 += __uint_as_float(y[i][2]); }
      s0 += dpp<ROR4>(s0); s0 += dpp<ROR8>(s0); s0 = x16_sum(s0); s0 = x32_sum(s0);
      s1 += dpp<ROR4>(s1); s1 += dpp<ROR8>(s1); s1 = x16_sum(s1); s1 = x32_sum(s1);
      if (lane < 4) reinterpret_cast<unsigned*>(out)[rid * 4 + lane] = pkbf(bflo(xres) + s0, bfhi(xres) + s1);
      if (rid == 0 && lane == 0) __builtin_amdgcn_raw_buffer_store_b32(epoch, rws, WS_EPOCH, 0, AUX_SC1);
      ST(7);
    }
    STFLUSH();
  }
}
}

extern "C" void kernel_launch(void* const* d_in, const int* in_sizes, int n_in, void* d_out, int out_size, void* d_ws, size_t ws_size, hipStream_t stream) {
  using fk::u16;
  fk::fused<<<256, 512, 0, stream>>>((const u16*)d_in[0], (const u16*)d_in[1], (const u16*)d_in[2], (const u16*)d_in[3], (const u16*)d_in[4],
                                     (const u16*)d_in[6], (const u16*)d_in[7], (const u16*)d_in[8], (u16*)d_out, (unsigned char*)d_ws);
}
```

```cpp
#include <hip/hip_runtime.h>
#include <hip/hip_bf16.h>
#include <stdint.h>

namespace fk {
typedef unsigned short u16;
typedef unsigned u32x4 __attribute__((ext_vector_type(4)));
typedef unsigned u32x2 __attribute__((ext_vector_type(2)));
typedef unsigned u32x3 __attribute__((ext_vector_type(3)));
typedef float f32x4 __attribute__((ext_vector_type(4)));
typedef __bf16 bf16x8 __attribute__((ext_vector_type(8)));
typedef __bf16 bf16x2 __attribute__((ext_vector_type(2)));
#define LAS __attribute__((address_space(3)))
#define SB() __builtin_amdgcn_sched_barrier(0)

constexpr int D = 576, SK = 32768, DH = 128, F = 4608, NQ = 4096;
#define POOL 24
constexpr int CHUNK_B = 8192;
constexpr int NSTC = 128 - POOL, NSTW = NSTC / 4;
constexpr int NTK = 8 * POOL;
constexpr int GROUP_B = SK * DH * 2;
static_assert(NSTC % 4 == 0 && NSTW >= 3, "");
constexpr int WS_EPOCH = 0, WS_CNT = 4096, WS_QP = 16384, WS_PART = WS_QP + 16 * 2 * 4 * 256 * 8, WS_ML = WS_PART + 32 * 8 * 256 * 8,
              WS_Y = WS_ML + 32 * 8 * 2 * 16, WS_END = WS_Y + 144 * 128 * 64, WS_STAMP = 4 << 20;
constexpr int AUX_NT = 2, AUX_SC1 = 16, RSRC3 = 0x00020000;
constexpr unsigned SPIN_CAP = 1u << 18;
constexpr int L_XN = 0;
constexpr int L_QRED = L_XN + 2 * D;
constexpr int L_Q = L_QRED + 4 * 128;
constexpr int L_GU = L_Q + 128;
constexpr int L_H = L_GU + 2 * 3 * 64;
constexpr int L_ACC = L_H + 64;
constexpr int L_ML = L_ACC + 4 * 256;
constexpr int L_FLAG = L_ML + 16;
constexpr int L_DUMMY = L_FLAG + 16;
constexpr int L_SC = L_DUMMY + 64;
constexpr int L_WORDS = L_SC + (NSTC + 2) * 64;
#define NSLOT 2
#define NEXTRA 2
#define FFN_T 3
#define VMW(n) __builtin_amdgcn_s_waitcnt(0x0F70 | ((n) & 15) | (((n) >> 4) << 14))
#define GREQ 4
#define FFN_EARLY 0
#define PRE_T 8
static_assert(NSLOT == 2 && NEXTRA % 2 == 0 && NSTC % 2 == 0, "");

__device__ __forceinline__ float bflo(unsigned v) { return __uint_as_float(v << 16); }
__device__ __forceinline__ float bfhi(unsigned v) { return __uint_as_float(v & 0xffff0000u); }
__device__ __forceinline__ unsigned pkbf(float lo, float hi) { bf16x2 r = {(__bf16)lo, (__bf16)hi}; return __builtin_bit_cast(unsigned, r); }
__device__ __forceinline__ float rbf(float f) { return (float)(__bf16)f; }
__device__ __forceinline__ float fdiv(float a, float d, float r) { const float q = a * r; return fmaf(fmaf(-q, d, a), r, q); }
template <int CTRL> __device__ __forceinline__ float dpp(float x) {
  return __builtin_bit_cast(float, __builtin_amdgcn_mov_dpp(__builtin_bit_cast(int, x), CTRL, 0xf, 0xf, true));
}
template <int CTRL> __device__ __forceinline__ unsigned dppu(unsigned x) { return (unsigned)__builtin_amdgcn_mov_dpp((int)x, CTRL, 0xf, 0xf, true); }
template <int CTRL, int BANK> __device__ __forceinline__ float dpp_bank(float old, float x) {
  return __builtin_bit_cast(float, __builtin_amdgcn_update_dpp(__builtin_bit_cast(int, old), __builtin_bit_cast(int, x), CTRL, 0xf, BANK, false));
}
constexpr int XOR1 = 0xB1, XOR2 = 0x4E, ROR4 = 0x124, ROR8 = 0x128, ROR12 = 0x12C, IDENT = 0xE4;
__device__ __forceinline__ float x16_max(float x) { auto s = __builtin_amdgcn_permlane16_swap(__float_as_uint(x), __float_as_uint(x), false, false); return fmaxf(__uint_as_float(s[0]), __uint_as_float(s[1])); }
__device__ __forceinline__ float x32_max(float x) { auto s = __builtin_amdgcn_permlane32_swap(__float_as_uint(x), __float_as_uint(x), false, false); return fmaxf(__uint_as_float(s[0]), __uint_as_float(s[1])); }
__device__ __forceinline__ float x16_sum(float x) { auto s = __builtin_amdgcn_permlane16_swap(__float_as_uint(x), __float_as_uint(x), false, false); return __uint_as_float(s[0]) + __uint_as_float(s[1]); }
__device__ __forceinline__ float x32_sum(float x) { auto s = __builtin_amdgcn_permlane32_swap(__float_as_uint(x), __float_as_uint(x), false, false); return __uint_as_float(s[0]) + __uint_as_float(s[1]); }
__device__ __forceinline__ float t32_sum(float a, float b) { auto s = __builtin_amdgcn_permlane32_swap(__float_as_uint(a), __float_as_uint(b), false, false); return __uint_as_float(s[0]) + __uint_as_float(s[1]); }
__device__ __forceinline__ float t16_sum(float a, float b) { auto s = __builtin_amdgcn_permlane16_swap(__float_as_uint(a), __float_as_uint(b), false, false); return __uint_as_float(s[0]) + __uint_as_float(s[1]); }
__device__ __forceinline__ f32x4 mfma(u32x4 a, u32x4 b, f32x4 c) {
  return __builtin_amdgcn_mfma_f32_16x16x32_bf16(__builtin_bit_cast(bf16x8, a), __builtin_bit_cast(bf16x8, b), c, 0, 0, 0);
}
__device__ __forceinline__ unsigned perm_lo(unsigned odd, unsigned even) { return __builtin_amdgcn_perm(odd, even, 0x05040100u); }
__device__ __forceinline__ unsigned perm_hi(unsigned odd, unsigned even) { return __builtin_amdgcn_perm(odd, even, 0x07060302u); }
typedef __amdgpu_buffer_rsrc_t rsrc_t;
__device__ __forceinline__ rsrc_t mkrs(const void* p, int bytes) { return __builtin_amdgcn_make_buffer_rsrc((void*)p, 0, bytes, RSRC3); }

#define ST(i) do {} while (0)
#define STDECL
#define STFLUSH() do {} while (0)

__global__ void __launch_bounds__(512, 2) fused(const u16* __restrict__ x, const u16* __restrict__ kc, const u16* __restrict__ vc, const u16* __restrict__ wq,
                                                const u16* __restrict__ wo, const u16* __restrict__ wg, const u16* __restrict__ wu, const u16* __restrict__ wd,
                                                u16* out, unsigned char* ws) {
  __shared__ __attribute__((aligned(16))) float lds[L_WORDS];
  volatile LAS unsigned* flag = (volatile LAS unsigned*)(LAS float*)(lds + L_FLAG);
  const int tid = threadIdx.x, lane = tid & 63, wave8 = __builtin_amdgcn_readfirstlane(tid >> 6), wave = wave8 & 3;
  const int g = blockIdx.x >> 3, s = blockIdx.x & 7, b = g >> 4, kvh = g & 15;
  const rsrc_t rws = mkrs(ws, WS_END);
  constexpr float SCALE = 0.088388347648318440f, LOG2E = 1.4426950408889634f;
  STDECL;
  ST(0);
  if (wave8 >= 4) {
    const u16* kb = kc + (size_t)g * (SK * DH); const u16* vb = vc + (size_t)g * (SK * DH);
    const int voffK = ((lane >> 2) & 3) * 256 + (lane >> 4) * 64 + (lane & 3) * 16, voffV = lane * 16;
    u32x4 R[NSLOT + NEXTRA][8];
#define LD1(d, u, rs, vo, so) R[d][u] = __builtin_amdgcn_raw_buffer_load_b128(rs, (vo) + ((u) & 3) * 1024, (so) + ((u) >> 2) * 4096, AUX_NT)
#define SOFF(i) ((s << 20) + ((((i) >> 1) * 4 + wave) * 2 + ((i) & 1)) * CHUNK_B)
    {
      const f32x4 sent = {__uint_as_float(~0u), __uint_as_float(~0u), __uint_as_float(~0u), __uint_as_float(~0u)};
#pragma unroll
      for (int i = 0; i < (NSTC + 15) / 16; ++i) if (i * 16 + wave * 4 + (lane >> 4) < NSTC) *reinterpret_cast<f32x4*>(lds + L_SC + i * 1024 + wave * 256 + lane * 4) = sent;
      if (wave < 2) lds[L_SC + (NSTC + wave) * 64 + lane] = -INFINITY;
      asm volatile("s_waitcnt lgkmcnt(0)" ::: "memory");
    }
    __builtin_amdgcn_s_barrier();
    {
      const rsrc_t rk = mkrs(kb, GROUP_B);
#pragma unroll
      for (int d = 0; d < NSLOT + NEXTRA; ++d)
#pragma unroll
        for (int u = 0; u < 8; ++u) {
          if (d * 8 + u >= PRE_T) VMW(PRE_T - 1);
          LD1(d, u, rk, voffK, SOFF(d)); SB(); }
    }
    ST(1);
    for (unsigned spin = 0; flag[1] == 0u && spin < (SPIN_CAP << 4); ++spin) __builtin_amdgcn_s_sleep(1);
    asm volatile("" ::: "memory");
    const unsigned epoch = flag[0];
    ST(2);
    const int n = lane & 15;
    const u32x4 qv = *reinterpret_cast<const u32x4*>(reinterpret_cast<const u16*>(lds + L_Q) + (n & 1) * 128 + (4 * (lane >> 4) + (n >> 2)) * 8);
    const u32x4 zero4 = {0u, 0u, 0u, 0u};
    const u32x4 Q0 = (n & 2) ? zero4 : qv, Q1 = (n & 2) ? qv : zero4;
    volatile LAS float* scw = (volatile LAS float*)(LAS float*)(lds + L_SC) + lane;
#define QK(d, rs, vo, so, SC, REFILL) do { \
      f32x4 dd[4]; \
      _Pragma("unroll") for (int p = 0; p < 4; ++p) { dd[p] = mfma(R[d][2 * p], Q0, f32x4{0.f, 0.f, 0.f, 0.f}); dd[p] = mfma(R[d][2 * p + 1], Q1, dd[p]); SB(); \
        if (REFILL) { LD1(d, 2 * p, rs, vo, so); SB(); LD1(d, 2 * p + 1, rs, vo, so); SB(); } } \
      float x0 = dpp<IDENT>(dd[0][0]), x1 = dpp<ROR12>(dd[0][1]), x2 = dpp<ROR8>(dd[0][2]), x3 = dpp<ROR4>(dd[0][3]); \
      x0 = dpp_bank<ROR4, 2>(x0, dd[1][0]); x1 = dpp_bank<IDENT, 2>(x1, dd[1][1]); x2 = dpp_bank<ROR12, 2>(x2, dd[1][2]); x3 = dpp_bank<ROR8, 2>(x3, dd[1][3]); \
      x0 = dpp_bank<ROR8, 4>(x0, dd[2][0]); x1 = dpp_bank<ROR4, 4>(x1, dd[2][1]); x2 = dpp_bank<IDENT, 4>(x2, dd[2][2]); x3 = dpp_bank<ROR12, 4>(x3, dd[2][3]); \
      x0 = dpp_bank<ROR12, 8>(x0, dd[3][0]); x1 = dpp_bank<ROR8, 8>(x1, dd[3][1]); x2 = dpp_bank<ROR4, 8>(x2, dd[3][2]); x3 = dpp_bank<IDENT, 8>(x3, dd[3][3]); \
      SC = rbf(rbf((x0 + x1) + (x2 + x3)) * SCALE); } while (0)
#define PV(d, rs, vo, so, SC) do { \
      const float pp = __builtin_amdgcn_exp2f(((SC) - m) * LOG2E); \
      l += pp; \
      const unsigned w_ = pkbf(pp, dpp<0x102>(pp)); \
      const u32x4 PA = {w_, dppu<0x104>(w_), dppu<0x108>(w_), dppu<0x10C>(w_)}; \
      u32x4 bf[8]; \
      SB(); \
      _Pragma("unroll") for (int t = 0; t < 4; ++t) { \
        _Pragma("unroll") for (int c = 0; c < 4; ++c) { bf[2 * c][t] = perm_lo(R[d][2 * t + 1][c], R[d][2 * t][c]); bf[2 * c + 1][t] = perm_hi(R[d][2 * t + 1][c], R[d][2 * t][c]); } \
        SB(); LD1(d, 2 * t, rs, vo, so); SB(); LD1(d, 2 * t + 1, rs, vo, so); SB(); } \
      _Pragma("unroll") for (int j = 0; j < 8; ++j) acc[j] = mfma(PA, bf[j], acc[j]); \
      SB(); } while (0)
    constexpr int NP = NSTC / 2;
    LAS unsigned* ltp = (LAS unsigned*)(LAS float*)(lds + (lane == 0 ? L_FLAG + 8 : L_DUMMY + lane));
#define LTICKET() __hip_atomic_fetch_add(ltp, 1u, __ATOMIC_RELAXED, __HIP_MEMORY_SCOPE_WORKGROUP)
    float m = -3.0e38f;
    int cp = wave;
    unsigned lt = LTICKET();
    bool first = true, more;
    do {
      const int t = __builtin_amdgcn_readfirstlane((int)lt);
      lt = LTICKET();
      more = t < NP;
      const int np = more ? t : t - NP;
      const rsrc_t rs = mkrs(more ? kb : vb, GROUP_B);
      const int vo = more ? voffK : voffV, so = np < NP ? (s << 20) + np * (2 * CHUNK_B) : GROUP_B;
      SB();
#pragma unroll
      for (int d = 0; d < 2; ++d) { float sc; QK(d, rs, vo, so + d * CHUNK_B, sc, true); scw[(cp * 2 + d) * 64] = sc; m = fmaxf(m, sc); SB(); }
      if (NEXTRA && first) {
#pragma unroll
        for (int d = 2; d < 2 + NEXTRA; ++d) { float sc; QK(d, rs, 0, 0, sc, false); scw[(((d >> 1) * 4 + wave) * 2 + (d & 1)) * 64] = sc; m = fmaxf(m, sc); SB(); }
        first = false;
      }
      cp = np;
    } while (more);
    ST(3);
    m = fmaxf(m, dpp<XOR2>(m)); m = fmaxf(m, dpp<ROR4>(m)); m = fmaxf(m, dpp<ROR8>(m)); m = x16_max(m); m = x32_max(m);
    const rsrc_t rc = mkrs(ws + WS_CNT + g * 256, 4);
    const int aoff = lane == 0 ? 0 : 64;
    f32x4 acc[8];
#pragma unroll
    for (int j = 0; j < 8; ++j) acc[j] = f32x4{0.f, 0.f, 0.f, 0.f};
    float l = 0.f;
    int tk = 0, sop = GROUP_B;
    bool req = false;
#define RESCALE(SC) do { if (__any((SC) > m)) { \
        float pm = (SC); pm = fmaxf(pm, dpp<XOR2>(pm)); pm = fmaxf(pm, dpp<ROR4>(pm)); pm = fmaxf(pm, dpp<ROR8>(pm)); pm = x16_max(pm); pm = x32_max(pm); \
        const float mn = fmaxf(m, pm), al = __builtin_amdgcn_exp2f((m - mn) * LOG2E); \
        const float al0 = dpp<0x00>(al), al1 = dpp<0x55>(al); \
        l *= al; m = mn; \
        _Pragma("unroll") for (int j = 0; j < 8; ++j) { acc[j][0] *= al0; acc[j][1] *= al1; } } } while (0)
#define GTICKET() __builtin_amdgcn_raw_ptr_buffer_atomic_add_i32(1, rc, aoff, 0, 0)
#define POOLOFF(T) ((T) < (unsigned)NTK ? (int)((((T) & 7) << 20) + (NSTC + ((T) >> 3)) * CHUNK_B) : GROUP_B)
    do {
      const int t = __builtin_amdgcn_readfirstlane((int)lt);
      lt = LTICKET();
      more = t < 2 * NP;
      const int row = cp < NP ? cp * 2 : NSTC;
      float sc0 = scw[row * 64], sc1 = scw[row * 64 + 64];
      if (POOL && t >= 2 * NP - GREQ) {
        if (!req) { tk = GTICKET(); req = true; }
        if (!more) { const unsigned t_ = (unsigned)__builtin_amdgcn_readfirstlane(tk); sop = POOLOFF(t_); tk = GTICKET(); }
      }
      const rsrc_t rs0 = mkrs(more ? vb : kb, GROUP_B), rs1 = mkrs(vb, GROUP_B);
      const int vo0 = more ? voffV : voffK, so0 = more ? (s << 20) + (t - NP) * (2 * CHUNK_B) : sop, so1 = more ? so0 + CHUNK_B : sop;
      for (unsigned spin = 0; __any(__float_as_uint(sc0) == ~0u || __float_as_uint(sc1) == ~0u) && spin < (SPIN_CAP << 4); ++spin) { sc0 = scw[row * 64]; sc1 = scw[row * 64 + 64]; }
      const float scm = fmaxf(sc0, sc1);
      RESCALE(scm);
      SB();
      PV(0, rs0, vo0, so0, sc0);
      PV(1, rs1, voffV, so1, sc1);
      cp = t - NP;
    } while (more);
    ST(6);
    if (POOL) {
      const rsrc_t rk = mkrs(kb, GROUP_B), rv = mkrs(vb, GROUP_B);
      while (sop < GROUP_B) {
        const unsigned t_ = (unsigned)__builtin_amdgcn_readfirstlane(tk);
        sop = POOLOFF(t_);
        tk = __builtin_amdgcn_raw_ptr_buffer_atomic_add_i32(1, rc, aoff, sop < GROUP_B ? 0 : 64, 0);
        SB();
        float sc;
        QK(0, rk, voffK, sop, sc, true);
        RESCALE(sc);
        PV(1, rv, voffV, sop, sc);
      }
    }
    ST(4);
    l += dpp<XOR2>(l); l += dpp<ROR4>(l); l += dpp<ROR8>(l); l = x16_sum(l); l = x32_sum(l);
    if (lane < 16) {
#pragma unroll
      for (int h = 0; h < 2; ++h) {
        *reinterpret_cast<f32x4*>(lds + L_ACC + wave * 256 + h * 128 + lane * 8) = f32x4{acc[0][h], acc[1][h], acc[2][h], acc[3][h]};
        *reinterpret_cast<f32x4*>(lds + L_ACC + wave * 256 + h * 128 + lane * 8 + 4) = f32x4{acc[4][h], acc[5][h], acc[6][h], acc[7][h]};
      }
      if (lane < 2) *reinterpret_cast<float2*>(lds + L_ML + (wave * 2 + lane) * 2) = make_float2(m, l);
    }
    asm volatile("s_waitcnt lgkmcnt(0)" ::: "memory");
    unsigned old = 0;
    if (lane == 0) old = __hip_atomic_fetch_add((LAS unsigned*)&flag[2], 1u, __ATOMIC_RELAXED, __HIP_MEMORY_SCOPE_WORKGROUP);
    old = __builtin_amdgcn_readfirstlane(old);
    if (old == 3u) {
      asm volatile("" ::: "memory");
      const int h = lane >> 5;
      float mw[4], lw[4];
#pragma unroll
      for (int w = 0; w < 4; ++w) { const float2 t = *reinterpret_cast<const float2*>(lds + L_ML + (w * 2 + h) * 2); mw[w] = t.x; lw[w] = t.y; }
      const float M = fmaxf(fmaxf(mw[0], mw[1]), fmaxf(mw[2], mw[3]));
      float L = 0.f; f32x4 o = {0.f, 0.f, 0.f, 0.f};
#pragma unroll
      for (int w = 0; w < 4; ++w) {
        const float wt = __builtin_amdgcn_exp2f((mw[w] - M) * LOG2E);
        L = fmaf(wt, lw[w], L);
        o += wt * *reinterpret_cast<const f32x4*>(lds + L_ACC + w * 256 + lane * 4);
      }
      const int po = WS_PART + ((g * 8 + s) * 256 + lane * 4) * 8;
      __builtin_amdgcn_raw_buffer_store_b128(u32x4{__float_as_uint(o[0]), epoch, __float_as_uint(o[1]), epoch}, rws, po, 0, AUX_SC1);
      __builtin_amdgcn_raw_buffer_store_b128(u32x4{__float_as_uint(o[2]), epoch, __float_as_uint(o[3]), epoch}, rws, po + 16, 0, AUX_SC1);
      if ((lane & 31) == 0) __builtin_amdgcn_raw_buffer_store_b128(u32x4{__float_as_uint(M), epoch, __float_as_uint(L), epoch}, rws, WS_ML + ((g * 8 + s) * 2 + h) * 16, 0, AUX_SC1);
      if (lane == 0) flag[3] = 1u;
      ST(5);
    }
    STFLUSH();
  } else {
    __builtin_amdgcn_s_setprio(3);
    const int hw = wave;
    const rsrc_t rwq = mkrs(wq, D * NQ * 2), rx = mkrs(x, 2 * D * 2);
    const int cb = s & 3, rq = (s >> 2) + 2 * b;
    const int qrow0 = 144 * rq + 36 * hw + (lane >> 3);
    const unsigned epoch = __builtin_amdgcn_raw_buffer_load_b32(rws, WS_EPOCH, 0, AUX_SC1) + 1u;
    SB();
    u32x3 xr[3];
#pragma unroll
    for (int i = 0; i < 3; ++i) xr[i] = __builtin_amdgcn_raw_buffer_load_b96(rx, lane * 36 + 12 * i, 0, 0);
    u32x4 wqr[5];
#pragma unroll
    for (int i = 0; i < 5; ++i) wqr[i] = __builtin_amdgcn_raw_buffer_load_b128(rwq, (i == 4 && lane >= 32) ? 0x7ffffff0 : (qrow0 + 8 * i) * (NQ * 2) + (kvh * 256 + cb * 64 + (lane & 7) * 8) * 2, 0, AUX_NT);
    __builtin_amdgcn_raw_buffer_store_b32(0u, rws, (s == 0 && tid == 0) ? WS_CNT + g * 256 : 0x7ffffff0, 0, AUX_SC1);
    if (tid < 16) flag[lane] = lane == 8 ? 2u * (NSLOT + NEXTRA) : 0u;
    asm volatile("s_waitcnt lgkmcnt(0)" ::: "memory");
    SB();
    __builtin_amdgcn_s_barrier();
    ST(1);
    const rsrc_t rwg = mkrs(wg, hw ? D * F * 2 : 0), rwu = mkrs(wu, hw ? D * F * 2 : 0), rwd = mkrs(wd, hw ? F * D * 2 : 0), rwo = mkrs(wo, NQ * D * 2);
    const int gq = lane >> 4, c = lane & 15;
    const int colA = (hw - 1) * 192 + 8 * c, colB = (hw - 1) * 192 + 128 + 4 * c;
    const int frow0 = 192 * (hw - 1) + (lane >> 2);
    u32x4 gw[12], uw[12], wa[8]; u32x2 wb[8];
#define LDW(rs, row0) _Pragma("unroll") for (int j = 0; j < 8; ++j) { \
      VMW(FFN_T - 1); wa[j] = __builtin_amdgcn_raw_buffer_load_b128(rs, (8 * gq + j) * (D * 2) + colA * 2, (row0) * (D * 2), AUX_NT); SB(); \
      VMW(FFN_T - 1); wb[j] = __builtin_amdgcn_raw_buffer_load_b64(rs, (8 * gq + j) * (D * 2) + colB * 2, (row0) * (D * 2), AUX_NT); SB(); } SB()
#define LDGU(u0) _Pragma("unroll") for (int i = 0; i < 12; ++i) { \
      VMW(FFN_T - 1); gw[i] = __builtin_amdgcn_raw_buffer_load_b128(rwg, frow0 * (F * 2) + (lane & 3) * 16, 16 * i * (F * 2) + (u0) * 2, AUX_NT); SB(); \
      VMW(FFN_T - 1); uw[i] = __builtin_amdgcn_raw_buffer_load_b128(rwu, frow0 * (F * 2) + (lane & 3) * 16, 16 * i * (F * 2) + (u0) * 2, AUX_NT); SB(); } SB()
    {
      float v[18];
#pragma unroll
      for (int i = 0; i < 3; ++i)
#pragma unroll
        for (int j = 0; j < 3; ++j) { v[6 * i + 2 * j] = bflo(xr[i][j]); v[6 * i + 2 * j + 1] = bfhi(xr[i][j]); }
      float sm = 0.f;
#pragma unroll
      for (int i = 0; i < 18; ++i) sm += v[i];
      sm += dpp<XOR1>(sm); sm += dpp<XOR2>(sm); sm += dpp<ROR4>(sm); sm += dpp<ROR8>(sm); sm = x16_sum(sm);
      const float mean = sm * (1.f / D);
      float ss = 0.f;
#pragma unroll
      for (int i = 0; i < 18; ++i) { const float d = v[i] - mean; ss = fmaf(d, d, ss); }
      ss += dpp<XOR1>(ss); ss += dpp<XOR2>(ss); ss += dpp<ROR4>(ss); ss += dpp<ROR8>(ss); ss = x16_sum(ss);
      const float mb = rbf(mean), den = rbf(sqrtf(rbf(rbf(ss * (1.f / D)) + 1e-5f))), rden = 1.f / den;
#pragma unroll
      for (int i = 0; i < 18; i += 2) {
        float2 o; o.x = rbf(fdiv(rbf(v[i] - mb), den, rden)); o.y = rbf(fdiv(rbf(v[i + 1] - mb), den, rden));
        *reinterpret_cast<float2*>(lds + L_XN + lane * 18 + i) = o;
      }
      asm volatile("s_waitcnt lgkmcnt(0)" ::: "memory");
      float a0[8], a1[8];
#pragma unroll
      for (int j = 0; j < 8; ++j) { a0[j] = 0.f; a1[j] = 0.f; }
#pragma unroll
      for (int i = 0; i < 5; ++i) {
        const int rr = i == 4 ? min(qrow0 + 32, D - 1) : qrow0 + 8 * i;
        const float x0 = lds[L_XN + rr], x1 = lds[L_XN + D + rr];
#pragma unroll
        for (int j = 0; j < 4; ++j) {
          const float lo = bflo(wqr[i][j]), hi = bfhi(wqr[i][j]);
          a0[2 * j] = fmaf(x0, lo, a0[2 * j]); a0[2 * j + 1] = fmaf(x0, hi, a0[2 * j + 1]);
          a1[2 * j] = fmaf(x1, lo, a1[2 * j]); a1[2 * j + 1] = fmaf(x1, hi, a1[2 * j + 1]);
        }
      }
      float r1[8], r2[4];
#pragma unroll
      for (int j = 0; j < 8; ++j) r1[j] = t32_sum(a0[j], a1[j]);
#pragma unroll
      for (int j = 0; j < 4; ++j) { r2[j] = t16_sum(r1[j], r1[4 + j]); r2[j] += dpp<ROR8>(r2[j]); }
      if ((lane & 8) == 0) *reinterpret_cast<f32x4*>(lds + L_QRED + hw * 128 + (lane >> 5) * 64 + (lane & 7) * 8 + ((lane >> 4) & 1) * 4) = f32x4{r2[0], r2[1], r2[2], r2[3]};
      asm volatile("s_waitcnt lgkmcnt(0)" ::: "memory");
      if (lane == 0) __hip_atomic_fetch_add((LAS unsigned*)&flag[6], 1u, __ATOMIC_RELAXED, __HIP_MEMORY_SCOPE_WORKGROUP);
    }
    ST(2);
#define YMFMA(AF) do { \
      f32x4 ya[8], yb[4]; const f32x4 z4 = {0.f, 0.f, 0.f, 0.f}; \
      _Pragma("unroll") for (int cc = 0; cc < 4; ++cc) { \
        const u32x4 blo = {perm_lo(wa[1][cc], wa[0][cc]), perm_lo(wa[3][cc], wa[2][cc]), perm_lo(wa[5][cc], wa[4][cc]), perm_lo(wa[7][cc], wa[6][cc])}; \
        const u32x4 bhi = {perm_hi(wa[1][cc], wa[0][cc]), perm_hi(wa[3][cc], wa[2][cc]), perm_hi(wa[5][cc], wa[4][cc]), perm_hi(wa[7][cc], wa[6][cc])}; \
        ya[2 * cc] = mfma(AF, blo, z4); ya[2 * cc + 1] = mfma(AF, bhi, z4); } \
      _Pragma("unroll") for (int cc = 0; cc < 2; ++cc) { \
        const u32x4 blo = {perm_lo(wb[1][cc], wb[0][cc]), perm_lo(wb[3][cc], wb[2][cc]), perm_lo(wb[5][cc], wb[4][cc]), perm_lo(wb[7][cc], wb[6][cc])}; \
        const u32x4 bhi = {perm_hi(wb[1][cc], wb[0][cc]), perm_hi(wb[3][cc], wb[2][cc]), perm_hi(wb[5][cc], wb[4][cc]), perm_hi(wb[7][cc], wb[6][cc])}; \
        yb[2 * cc] = mfma(AF, blo, z4); yb[2 * cc + 1] = mfma(AF, bhi, z4); } \
      const float a0_ = (gq & 1) ? ya[2][0] : ya[0][0], a1_ = (gq & 1) ? ya[6][0] : ya[4][0]; e0 += (gq & 2) ? a1_ : a0_; \
      const float b0_ = (gq & 1) ? ya[3][0] : ya[1][0], b1_ = (gq & 1) ? ya[7][0] : ya[5][0]; e1 += (gq & 2) ? b1_ : b0_; \
      const float c0_ = (gq & 1) ? yb[1][0] : yb[0][0], c1_ = (gq & 1) ? yb[3][0] : yb[2][0]; e2 += (gq & 2) ? c1_ : c0_; } while (0)
    if (hw) {
      float e0 = 0.f, e1 = 0.f, e2 = 0.f;
      const int nunit = ((kvh >> 1) == s) ? 2 : 1;
      for (unsigned spin = 0; flag[1] == 0u && spin < (SPIN_CAP << 4); ++spin) __builtin_amdgcn_s_sleep(2);
#pragma unroll 1
      for (int it = 0; it < nunit; ++it) {
        const int u0 = (s * 18 + (it == 0 ? kvh : 16 + (kvh & 1))) * 32;
        LDGU(u0); LDW(rwd, u0);
        float ag[8], au[8];
#pragma unroll
        for (int j = 0; j < 8; ++j) { ag[j] = 0.f; au[j] = 0.f; }
#pragma unroll
        for (int i = 0; i < 12; ++i) {
          const float xv = lds[L_XN + b * D + frow0 + 16 * i];
#pragma unroll
          for (int j = 0; j < 4; ++j) {
            ag[2 * j] = fmaf(xv, bflo(gw[i][j]), ag[2 * j]); ag[2 * j + 1] = fmaf(xv, bfhi(gw[i][j]), ag[2 * j + 1]);
            au[2 * j] = fmaf(xv, bflo(uw[i][j]), au[2 * j]); au[2 * j + 1] = fmaf(xv, bfhi(uw[i][j]), au[2 * j + 1]);
          }
        }
        float r1[8], r2[4];
#pragma unroll
        for (int j = 0; j < 8; ++j) r1[j] = t32_sum(ag[j], au[j]);
#pragma unroll
        for (int j = 0; j < 4; ++j) { r2[j] = t16_sum(r1[j], r1[4 + j]); r2[j] += dpp<ROR4>(r2[j]); r2[j] += dpp<ROR8>(r2[j]); }
        if ((lane & 12) == 0) *reinterpret_cast<f32x4*>(lds + L_GU + (it * 3 + hw - 1) * 64 + (lane >> 5) * 32 + (lane & 3) * 8 + ((lane >> 4) & 1) * 4) = f32x4{r2[0], r2[1], r2[2], r2[3]};
        asm volatile("s_waitcnt lgkmcnt(0)" ::: "memory");
        if (lane == 0) __hip_atomic_fetch_add((LAS unsigned*)&flag[4 + it], 1u, __ATOMIC_RELAXED, __HIP_MEMORY_SCOPE_WORKGROUP);
        for (unsigned spin = 0; flag[4 + it] < 3u && spin < (SPIN_CAP << 4); ++spin) __builtin_amdgcn_s_sleep(1);
        asm volatile("" ::: "memory");
        const int cc = lane & 31;
        const float* gu = lds + L_GU + it * 192 + cc;
        const float gt = rbf((gu[0] + gu[64]) + gu[128]), up = rbf((gu[32] + gu[96]) + gu[160]);
        const float y2 = -2.f * 0.7978845608028654f * LOG2E * (gt + 0.044715f * gt * gt * gt);
        const float ge = rbf(gt / (1.f + __builtin_amdgcn_exp2f(y2)));
        reinterpret_cast<__bf16*>(lds + L_H + hw * 16)[cc] = (__bf16)(ge * up);
        asm volatile("s_waitcnt lgkmcnt(0)" ::: "memory");
        const u32x4 HA = *reinterpret_cast<const u32x4*>(lds + L_H + hw * 16 + gq * 4);
        YMFMA(HA);
        SB();
      }
      ST(4);
      LDW(rwo, kvh * 256 + s * 32);
      for (unsigned spin = 0; flag[3] == 0u && spin < (SPIN_CAP << 4); ++spin) __builtin_amdgcn_s_sleep(4);
      ST(5);
      const int sp = (lane >> 1) & 7;
      const int po = WS_PART + ((g * 8 + sp) * 256 + s * 32 + gq * 8 + (lane & 1) * 4) * 8, mo = WS_ML + ((g * 8 + sp) * 2 + (s >> 2)) * 16;
      u32x4 p0, p1, ml;
      for (unsigned spin = 0;; ++spin) {
        asm volatile("" ::: "memory");
        p0 = __builtin_amdgcn_raw_buffer_load_b128(rws, po, 0, AUX_SC1);
        p1 = __builtin_amdgcn_raw_buffer_load_b128(rws, po + 16, 0, AUX_SC1);
        ml = __builtin_amdgcn_raw_buffer_load_b128(rws, mo, 0, AUX_SC1);
        const bool ok = p0[1] == epoch && p0[3] == epoch && p1[1] == epoch && p1[3] == epoch && ml[1] == epoch && ml[3] == epoch;
        if (__all(ok) || spin > SPIN_CAP) break;
      }
      ST(6);
      const float ms = __uint_as_float(ml[0]);
      float M = ms; M = fmaxf(M, dpp<XOR2>(M)); M = fmaxf(M, dpp<ROR4>(M)); M = fmaxf(M, dpp<ROR8>(M));
      const float wt = __builtin_amdgcn_exp2f((ms - M) * LOG2E);
      float v[5] = {wt * __uint_as_float(p0[0]), wt * __uint_as_float(p0[2]), wt * __uint_as_float(p1[0]), wt * __uint_as_float(p1[2]), wt * __uint_as_float(ml[2])};
#pragma unroll
      for (int j = 0; j < 5; ++j) { v[j] += dpp<XOR2>(v[j]); v[j] += dpp<ROR4>(v[j]); v[j] += dpp<ROR8>(v[j]); }
      const float inv = 1.f / v[4];
      const unsigned o0 = pkbf(v[0] * inv, v[1] * inv), o1 = pkbf(v[2] * inv, v[3] * inv);
      const u32x4 OA = {dppu<0xA0>(o0), dppu<0xA0>(o1), dppu<0xF5>(o0), dppu<0xF5>(o1)};
      YMFMA(OA);
      const int prod = kvh * 8 + s;
      __builtin_amdgcn_raw_buffer_store_b128(u32x4{__float_as_uint(e0), epoch, __float_as_uint(e1), epoch}, rws,
                                             WS_Y + ((b * 72 + (colA >> 3)) * 128 + prod) * 64 + gq * 16, 0, AUX_SC1);
      __builtin_amdgcn_raw_buffer_store_b64(u32x2{__float_as_uint(e2), epoch}, rws,
                                            WS_Y + ((b * 72 + (colB >> 3)) * 128 + prod) * 64 + ((colB & 7) + gq) * 8, 0, AUX_SC1);
      if (lane == 0) __hip_atomic_fetch_add((LAS unsigned*)&flag[7], 1u, __ATOMIC_RELAXED, __HIP_MEMORY_SCOPE_WORKGROUP);
      ST(7);
    } else {
      asm volatile("s_waitcnt vmcnt(0)" ::: "memory");
      for (unsigned spin = 0; flag[6] < 4u && spin < (SPIN_CAP << 4); ++spin) {}
      asm volatile("" ::: "memory");
      {
        const float2 q0 = *reinterpret_cast<const float2*>(lds + L_QRED + 2 * lane), q1 = *reinterpret_cast<const float2*>(lds + L_QRED + 128 + 2 * lane);
        const float2 q2 = *reinterpret_cast<const float2*>(lds + L_QRED + 256 + 2 * lane), q3 = *reinterpret_cast<const float2*>(lds + L_QRED + 384 + 2 * lane);
        const float qa = (q0.x + q1.x) + (q2.x + q3.x), qb = (q0.y + q1.y) + (q2.y + q3.y);
        __builtin_amdgcn_raw_buffer_store_b128(u32x4{__float_as_uint(qa), epoch, __float_as_uint(qb), epoch}, rws,
                                               WS_QP + (((kvh * 2 + (lane >> 5)) * 4 + rq) * 256 + cb * 64 + 2 * (lane & 31)) * 8, 0, AUX_SC1);
      }
      ST(3);
      u32x4 p[8];
      for (unsigned spin = 0;; ++spin) {
        asm volatile("" ::: "memory");
        bool ok = true;
#pragma unroll
        for (int r = 0; r < 8; ++r) p[r] = __builtin_amdgcn_raw_buffer_load_b128(rws, WS_QP + (kvh * 2 + b) * 8192 + r * 1024 + lane * 16, 0, AUX_SC1);
#pragma unroll
        for (int r = 0; r < 8; ++r) ok = ok && p[r][1] == epoch && p[r][3] == epoch;
        if (__all(ok) || spin > SPIN_CAP) break;
      }
#pragma unroll
      for (int hh = 0; hh < 2; ++hh) {
        const float qa = (__uint_as_float(p[hh][0]) + __uint_as_float(p[2 + hh][0])) + (__uint_as_float(p[4 + hh][0]) + __uint_as_float(p[6 + hh][0]));
        const float qb = (__uint_as_float(p[hh][2]) + __uint_as_float(p[2 + hh][2])) + (__uint_as_float(p[4 + hh][2]) + __uint_as_float(p[6 + hh][2]));
        reinterpret_cast<unsigned*>(lds + L_Q)[hh * 64 + lane] = pkbf(qa, qb);
      }
      if (lane == 0) flag[0] = epoch;
      asm volatile("s_waitcnt lgkmcnt(0)" ::: "memory");
      if (lane == 0) flag[1] = 1u;
      ST(4);
      if (blockIdx.x < 144) {
        const int rid = blockIdx.x;
        const unsigned xres = __builtin_amdgcn_raw_buffer_load_b32(rx, rid * 16 + (lane & 3) * 4, 0, 0);
        for (unsigned spin = 0; flag[7] == 0u && spin < (SPIN_CAP << 4); ++spin) __builtin_amdgcn_s_sleep(4);
        ST(5);
        u32x4 y[8];
        for (unsigned spin = 0;; ++spin) {
          asm volatile("" ::: "memory");
          bool ok = true;
#pragma unroll
          for (int i = 0; i < 8; ++i) y[i] = __builtin_amdgcn_raw_buffer_load_b128(rws, WS_Y + rid * 8192 + i * 1024 + lane * 16, 0, AUX_SC1);
#pragma unroll
          for (int i = 0; i < 8; ++i) ok = ok && y[i][1] == epoch && y[i][3] == epoch;
          if (__all(ok) || spin > SPIN_CAP) break;
        }
        ST(6);
        float s0 = 0.f, s1 = 0.f;
#pragma unroll
        for (int i = 0; i < 8; ++i) { s0 += __uint_as_float(y[i][0]); s1 += __uint_as_float(y[i][2]); }
        s0 += dpp<ROR4>(s0); s0 += dpp<ROR8>(s0); s0 = x16_sum(s0); s0 = x32_sum(s0);
        s1 += dpp<ROR4>(s1); s1 += dpp<ROR8>(s1); s1 = x16_sum(s1); s1 = x32_sum(s1);
        if (lane < 4) reinterpret_cast<unsigned*>(out)[rid * 4 + lane] = pkbf(bflo(xres) + s0, bfhi(xres) + s1);
        if (rid == 0 && lane == 0) __builtin_amdgcn_raw_buffer_store_b32(epoch, rws, WS_EPOCH, 0, AUX_SC1);
        ST(7);
      }
    }
    STFLUSH();
  }
}
}

extern "C" void kernel_launch(void* const* d_in, const int* in_sizes, int n_in, void* d_out, int out_size, void* d_ws, size_t ws_size, hipStream_t stream) {
  using fk::u16;
  fk::fused<<<256, 512, 0, stream>>>((const u16*)d_in[0], (const u16*)d_in[1], (const u16*)d_in[2], (const u16*)d_in[3], (const u16*)d_in[4],
                                     (const u16*)d_in[6], (const u16*)d_in[7], (const u16*)d_in[8], (u16*)d_out, (unsigned char*)d_ws);
}
```

```cpp
#include <hip/hip_runtime.h>
#include <hip/hip_bf16.h>
#include <stdint.h>

namespace fk {
typedef unsigned short u16;
typedef unsigned u32x4 __attribute__((ext_vector_type(4)));
typedef unsigned u32x2 __attribute__((ext_vector_type(2)));
typedef unsigned u32x3 __attribute__((ext_vector_type(3)));
typedef float f32x4 __attribute__((ext_vector_type(4)));
typedef __bf16 bf16x8 __attribute__((ext_vector_type(8)));
typedef __bf16 bf16x2 __attribute__((ext_vector_type(2)));
#define LAS __attribute__((address_space(3)))
#define SB() __builtin_amdgcn_sched_barrier(0)

constexpr int D = 576, SK = 32768, DH = 128, F = 4608, NQ = 4096;
#define POOL 24
constexpr int CHUNK_B = 8192, PAIR_B = 16384;
constexpr int NP = (128 - POOL) / 2, LBASE = 2 * NP;
constexpr int NTK = 8 * POOL;
constexpr int LMAX = 56;
constexpr int GROUP_B = SK * DH * 2;
static_assert(POOL % 2 == 0 && POOL >= 2 && POOL <= 48, "");
constexpr int WS_EPOCH = 0, WS_CNT = 4096, WS_QP = 16384, WS_PART = WS_QP + 16 * 2 * 4 * 256 * 8, WS_ML = WS_PART + 32 * 8 * 256 * 8,
              WS_Y = WS_ML + 32 * 8 * 2 * 16, WS_END = WS_Y + 144 * 128 * 64, WS_STAMP = 8 << 20;
constexpr int AUX_NT = 2, AUX_SC1 = 16, RSRC3 = 0x00020000, OOB = 0x7f000000;
constexpr unsigned SPIN_CAP = 1u << 18;
constexpr int L_XN = 0;
constexpr int L_QRED = L_XN + 2 * D;
constexpr int L_Q = L_QRED + 4 * 128;
constexpr int L_GU = L_Q + 128;
constexpr int L_H = L_GU + 2 * 3 * 64;
constexpr int L_ACC = L_H + 64;
constexpr int L_ML = L_ACC + 4 * 256;
constexpr int L_FLAG = L_ML + 16;
constexpr int L_DUMMY = L_FLAG + 16;
constexpr int L_LIST = L_DUMMY + 64;
constexpr int L_SC = L_LIST + 64;
constexpr int L_WORDS = L_SC + 65 * 64;
#define NEXTRA 2
#define FFN_T 3
#define LEAD 0
#define PIPEPOLL 0
#define FFN_EARLY 0
#define YPRIO 3
#define CODEPF 0
#define LATE 0
#define LATEREM 0
#define LATEPRIO 0
#define POOL_T 16
#define MAIN_T 16
#define FULLLINE 1
#define MISSONLY 1
#define GSS 12
#define GSSA 6
#define EARLY 8
#define PRE_T 8
#define VMW(n) __builtin_amdgcn_s_waitcnt(0x0F70 | ((n) & 15) | (((n) >> 4) << 14))
static_assert(NEXTRA % 2 == 0, "");

__device__ __forceinline__ float bflo(unsigned v) { return __uint_as_float(v << 16); }
__device__ __forceinline__ float bfhi(unsigned v) { return __uint_as_float(v & 0xffff0000u); }
__device__ __forceinline__ unsigned pkbf(float lo, float hi) { bf16x2 r = {(__bf16)lo, (__bf16)hi}; return __builtin_bit_cast(unsigned, r); }
__device__ __forceinline__ float rbf(float f) { return (float)(__bf16)f; }
__device__ __forceinline__ float fdiv(float a, float d, float r) { const float q = a * r; return fmaf(fmaf(-q, d, a), r, q); }
template <int CTRL> __device__ __forceinline__ float dpp(float x) {
  return __builtin_bit_cast(float, __builtin_amdgcn_mov_dpp(__builtin_bit_cast(int, x), CTRL, 0xf, 0xf, true));
}
template <int CTRL> __device__ __forceinline__ unsigned dppu(unsigned x) { return (unsigned)__builtin_amdgcn_mov_dpp((int)x, CTRL, 0xf, 0xf, true); }
template <int CTRL, int BANK> __device__ __forceinline__ float dpp_bank(float old, float x) {
  return __builtin_bit_cast(float, __builtin_amdgcn_update_dpp(__builtin_bit_cast(int, old), __builtin_bit_cast(int, x), CTRL, 0xf, BANK, false));
}
constexpr int XOR1 = 0xB1, XOR2 = 0x4E, ROR4 = 0x124, ROR8 = 0x128, ROR12 = 0x12C, IDENT = 0xE4;
__device__ __forceinline__ float x16_max(float x) { auto s = __builtin_amdgcn_permlane16_swap(__float_as_uint(x), __float_as_uint(x), false, false); return fmaxf(__uint_as_float(s[0]), __uint_as_float(s[1])); }
__device__ __forceinline__ float x32_max(float x) { auto s = __builtin_amdgcn_permlane32_swap(__float_as_uint(x), __float_as_uint(x), false, false); return fmaxf(__uint_as_float(s[0]), __uint_as_float(s[1])); }
__device__ __forceinline__ float x16_sum(float x) { auto s = __builtin_amdgcn_permlane16_swap(__float_as_uint(x), __float_as_uint(x), false, false); return __uint_as_float(s[0]) + __uint_as_float(s[1]); }
__device__ __forceinline__ float x32_sum(float x) { auto s = __builtin_amdgcn_permlane32_swap(__float_as_uint(x), __float_as_uint(x), false, false); return __uint_as_float(s[0]) + __uint_as_float(s[1]); }
__device__ __forceinline__ float t32_sum(float a, float b) { auto s = __builtin_amdgcn_permlane32_swap(__float_as_uint(a), __float_as_uint(b), false, false); return __uint_as_float(s[0]) + __uint_as_float(s[1]); }
__device__ __forceinline__ float t16_sum(float a, float b) { auto s = __builtin_amdgcn_permlane16_swap(__float_as_uint(a), __float_as_uint(b), false, false); return __uint_as_float(s[0]) + __uint_as_float(s[1]); }
__device__ __forceinline__ f32x4 mfma(u32x4 a, u32x4 b, f32x4 c) {
  return __builtin_amdgcn_mfma_f32_16x16x32_bf16(__builtin_bit_cast(bf16x8, a), __builtin_bit_cast(bf16x8, b), c, 0, 0, 0);
}
__device__ __forceinline__ unsigned perm_lo(unsigned odd, unsigned even) { return __builtin_amdgcn_perm(odd, even, 0x05040100u); }
__device__ __forceinline__ unsigned perm_hi(unsigned odd, unsigned even) { return __builtin_amdgcn_perm(odd, even, 0x07060302u); }
typedef __amdgpu_buffer_rsrc_t rsrc_t;
__device__ __forceinline__ rsrc_t mkrs(const void* p, int bytes) { return __builtin_amdgcn_make_buffer_rsrc((void*)p, 0, bytes, RSRC3); }
#define LDSADD(idx) __hip_atomic_fetch_add((LAS unsigned*)&flag[idx], 1u, __ATOMIC_RELAXED, __HIP_MEMORY_SCOPE_WORKGROUP)
#define LGKM0() asm volatile("s_waitcnt lgkmcnt(0)" ::: "memory")
#define CFENCE() asm volatile("" ::: "memory")

#define PT() do {} while (0)
#define PTFLUSH() do {} while (0)
#define ST(i) do {} while (0)
#define STDECL
#define STFLUSH() do {} while (0)

__global__ void __launch_bounds__(512, 2) fused(const u16* __restrict__ x, const u16* __restrict__ kc, const u16* __restrict__ vc, const u16* __restrict__ wq,
                                                const u16* __restrict__ wo, const u16* __restrict__ wg, const u16* __restrict__ wu, const u16* __restrict__ wd,
                                                u16* out, unsigned char* ws) {
  __shared__ __attribute__((aligned(16))) float lds[L_WORDS];
  volatile LAS unsigned* flag = (volatile LAS unsigned*)(LAS float*)(lds + L_FLAG);
  volatile LAS unsigned* list = (volatile LAS unsigned*)(LAS float*)(lds + L_LIST);
  const int tid = threadIdx.x, lane = tid & 63, wave8 = __builtin_amdgcn_readfirstlane(tid >> 6), wave = wave8 & 3;
  const int g = blockIdx.x >> 3, s = blockIdx.x & 7, b = g >> 4, kvh = g & 15;
  const rsrc_t rws = mkrs(ws, WS_END);
  constexpr float SCALE = 0.088388347648318440f, LOG2E = 1.4426950408889634f;
  const unsigned long long pc0 = __builtin_amdgcn_s_getpc();
  STDECL;
  ST(0);
  if (wave8 >= 4) {
    const u16* kb = kc + (size_t)g * (SK * DH); const u16* vb = vc + (size_t)g * (SK * DH);
    const int voffK = ((lane >> 2) & 3) * 256 + (lane >> 4) * 64 + (lane & 3) * 16, voffV = lane * 16;
    u32x4 R[2 + NEXTRA][8];
#define LD1(d, u, rs, vo, so) R[d][u] = __builtin_amdgcn_raw_buffer_load_b128(rs, (vo) + ((u) & 3) * 1024, (so) + ((u) >> 2) * 4096, AUX_NT)
    const rsrc_t rk0 = mkrs(kb, GROUP_B);
#define PREFILL(LO, HI) _Pragma("unroll") for (int i = (LO); i < (HI); ++i) { if (i >= PRE_T) VMW(PRE_T - 1); \
      LD1(i >> 3, i & 7, rk0, voffK, (s << 20) + ((i >> 4) * 4 + wave) * PAIR_B + ((i >> 3) & 1) * CHUNK_B); SB(); }
    PREFILL(0, EARLY)
    {
      const u32x4 sent = {~0u, ~0u, ~0u, ~0u};
#pragma unroll
      for (int i = 0; i < 4; ++i) *reinterpret_cast<u32x4*>(lds + L_SC + i * 1024 + wave * 256 + lane * 4) = sent;
      if (wave == 0) reinterpret_cast<unsigned*>(lds + L_SC)[64 * 64 + lane] = 0xff80ff80u;
      if (wave == 1) reinterpret_cast<unsigned*>(lds + L_LIST)[lane] = ~0u;
      LGKM0();
    }
    __builtin_amdgcn_s_barrier();
    PREFILL(EARLY, 8 * (2 + NEXTRA))
    ST(1);
    for (unsigned spin = 0; flag[1] == 0u && spin < (SPIN_CAP << 4); ++spin) __builtin_amdgcn_s_sleep(1);
    CFENCE();
    const unsigned epoch = flag[0];
    ST(2);
    const int n = lane & 15;
    const u32x4 qv = *reinterpret_cast<const u32x4*>(reinterpret_cast<const u16*>(lds + L_Q) + (n & 1) * 128 + (4 * (lane >> 4) + (n >> 2)) * 8);
    const u32x4 zero4 = {0u, 0u, 0u, 0u};
    const u32x4 Q0 = (n & 2) ? zero4 : qv, Q1 = (n & 2) ? qv : zero4;
    volatile LAS unsigned* scw = (volatile LAS unsigned*)(LAS float*)(lds + L_SC) + lane;
#define QK(d, rs, vo, so, SC, REFILL) QKT(d, rs, vo, so, SC, REFILL, MAIN_T)
#define QKT(d, rs, vo, so, SC, REFILL, T) do { \
      f32x4 dd[4]; \
      _Pragma("unroll") for (int p = 0; p < 4; ++p) { dd[p] = mfma(R[d][2 * p], Q0, f32x4{0.f, 0.f, 0.f, 0.f}); dd[p] = mfma(R[d][2 * p + 1], Q1, dd[p]); SB(); \
        if (REFILL) { if ((T) < 16) VMW((T) - 2); LD1(d, 2 * p, rs, vo, so); SB(); LD1(d, 2 * p + 1, rs, vo, so); SB(); } } \
      float x0 = dpp<IDENT>(dd[0][0]), x1 = dpp<ROR12>(dd[0][1]), x2 = dpp<ROR8>(dd[0][2]), x3 = dpp<ROR4>(dd[0][3]); \
      x0 = dpp_bank<ROR4, 2>(x0, dd[1][0]); x1 = dpp_bank<IDENT, 2>(x1, dd[1][1]); x2 = dpp_bank<ROR12, 2>(x2, dd[1][2]); x3 = dpp_bank<ROR8, 2>(x3, dd[1][3]); \
      x0 = dpp_bank<ROR8, 4>(x0, dd[2][0]); x1 = dpp_bank<ROR4, 4>(x1, dd[2][1]); x2 = dpp_bank<IDENT, 4>(x2, dd[2][2]); x3 = dpp_bank<ROR12, 4>(x3, dd[2][3]); \
      x0 = dpp_bank<ROR12, 8>(x0, dd[3][0]); x1 = dpp_bank<ROR8, 8>(x1, dd[3][1]); x2 = dpp_bank<ROR4, 8>(x2, dd[3][2]); x3 = dpp_bank<IDENT, 8>(x3, dd[3][3]); \
      SC = rbf((x0 + x1) + (x2 + x3)) * SCALE; } while (0)
#define PV(d, rs, vo, so, SC) PVT(d, rs, vo, so, SC, MAIN_T)
#define PVT(d, rs, vo, so, SC, T) do { \
      const float pp = __builtin_amdgcn_exp2f(((SC) - m) * LOG2E); \
      l += pp; \
      const unsigned w_ = pkbf(pp, dpp<0x102>(pp)); \
      const u32x4 PA = {w_, dppu<0x104>(w_), dppu<0x108>(w_), dppu<0x10C>(w_)}; \
      u32x4 bf[8]; \
      SB(); \
      _Pragma("unroll") for (int t_ = 0; t_ < 4; ++t_) { \
        _Pragma("unroll") for (int c = 0; c < 4; ++c) { bf[2 * c][t_] = perm_lo(R[d][2 * t_ + 1][c], R[d][2 * t_][c]); bf[2 * c + 1][t_] = perm_hi(R[d][2 * t_ + 1][c], R[d][2 * t_][c]); } \
        SB(); if ((T) < 16) VMW((T) - 2); LD1(d, 2 * t_, rs, vo, so); SB(); LD1(d, 2 * t_ + 1, rs, vo, so); SB(); } \
      _Pragma("unroll") for (int j = 0; j < 8; ++j) acc[j] = mfma(PA, bf[j], acc[j]); \
      SB(); } while (0)
    LAS unsigned* ltp = (LAS unsigned*)(LAS float*)(lds + (lane == 0 ? L_FLAG + 8 : L_DUMMY + lane));
#define LTICKET(inc) __hip_atomic_fetch_add(ltp, inc, __ATOMIC_RELAXED, __HIP_MEMORY_SCOPE_WORKGROUP)
    float m = -3.0e38f;
    int cp = wave, t;
    unsigned lt = LTICKET(1u);
    bool first = true;
    do {
      t = __builtin_amdgcn_readfirstlane((int)lt);
      lt = LTICKET(t < LBASE ? 1u : 0u);
      const bool nk = t < NP;
      const int np = nk ? t : (t < LBASE ? t - NP : 64);
      const rsrc_t rs = mkrs(nk ? kb : vb, GROUP_B);
      const int vo = nk ? voffK : voffV, so = np < 64 ? (s << 20) + np * PAIR_B : GROUP_B;
      SB();
      { float s0, s1; QK(0, rs, vo, so, s0, true); QK(1, rs, vo, so + CHUNK_B, s1, true);
        const unsigned pk = pkbf(s0, s1); scw[cp * 64] = pk; m = fmaxf(fmaxf(m, bflo(pk)), bfhi(pk)); SB(); }
      if (NEXTRA && first) {
#pragma unroll
        for (int d = 2; d < 2 + NEXTRA; d += 2) { float s0, s1; QK(d, rs, 0, 0, s0, false); QK(d + 1, rs, 0, 0, s1, false);
          const unsigned pk = pkbf(s0, s1); scw[((d >> 1) * 4 + wave) * 64] = pk; m = fmaxf(fmaxf(m, bflo(pk)), bfhi(pk)); SB(); PT(); }
        first = false;
      }
      cp = np;
      PT();
    } while (t < NP);
    if (t >= LBASE) lt = (unsigned)t;
    ST(3);
    m = fmaxf(m, dpp<XOR2>(m)); m = fmaxf(m, dpp<ROR4>(m)); m = fmaxf(m, dpp<ROR8>(m)); m = x16_max(m); m = x32_max(m);
    f32x4 acc[8];
#pragma unroll
    for (int j = 0; j < 8; ++j) acc[j] = f32x4{0.f, 0.f, 0.f, 0.f};
    float l = 0.f;
    const rsrc_t rv = mkrs(vb, GROUP_B);
#define RESCALE(SC) do { if (__any((SC) > m)) { \
        float pm = (SC); pm = fmaxf(pm, dpp<XOR2>(pm)); pm = fmaxf(pm, dpp<ROR4>(pm)); pm = fmaxf(pm, dpp<ROR8>(pm)); pm = x16_max(pm); pm = x32_max(pm); \
        const float mn = fmaxf(m, pm), al = __builtin_amdgcn_exp2f((m - mn) * LOG2E); \
        const float al0 = dpp<0x00>(al), al1 = dpp<0x55>(al); \
        l *= al; m = mn; \
        _Pragma("unroll") for (int j = 0; j < 8; ++j) { acc[j][0] *= al0; acc[j][1] *= al1; } } } while (0)
#define POOLITEM(SO) do { const int li = min(t - LBASE, 63); \
        unsigned e = (unsigned)__builtin_amdgcn_readfirstlane((int)list[li]); \
        for (unsigned spin = 0; e == ~0u && spin < (SPIN_CAP << 4); ++spin) e = (unsigned)__builtin_amdgcn_readfirstlane((int)list[li]); \
        SO = e < (unsigned)NTK ? (int)(((e & 7) << 20) + (2 * NP + (e >> 3)) * CHUNK_B) : GROUP_B; } while (0)
    int so;
    do {
      t = __builtin_amdgcn_readfirstlane((int)lt);
      lt = LTICKET(1u);
      unsigned pk = scw[cp * 64];
      const bool nv = t < LBASE;
      so = (s << 20) + (t - NP) * PAIR_B;
      if (!nv) POOLITEM(so);
      const rsrc_t rs0 = mkrs(nv ? vb : kb, GROUP_B);
      const int vo0 = nv ? voffV : voffK, so1 = nv ? so + CHUNK_B : so;
      for (unsigned spin = 0; __any(pk == ~0u) && spin < (SPIN_CAP << 4); ++spin) pk = scw[cp * 64];
      const float sc0 = bflo(pk), sc1 = bfhi(pk), scm = fmaxf(sc0, sc1);
      RESCALE(scm);
      SB();
      PV(0, rs0, vo0, so, sc0);
      PV(1, rv, voffV, so1, sc1);
      cp = t - NP;
      PT();
    } while (t < LBASE);
    ST(6);
    {
      const rsrc_t rk = mkrs(kb, GROUP_B);
      while (so < GROUP_B) {
        t = __builtin_amdgcn_readfirstlane((int)lt);
        lt = LTICKET(1u);
        POOLITEM(so);
        SB();
        float sc;
        QKT(0, rk, voffK, so, sc, true, POOL_T);
        sc = rbf(sc);
        RESCALE(sc);
        PVT(1, rv, voffV, so, sc, POOL_T);
        PT();
      }
    }
    ST(4);
    l += dpp<XOR2>(l); l += dpp<ROR4>(l); l += dpp<ROR8>(l); l = x16_sum(l); l = x32_sum(l);
    if (lane < 16) {
#pragma unroll
      for (int h = 0; h < 2; ++h) {
        *reinterpret_cast<f32x4*>(lds + L_ACC + wave * 256 + h * 128 + lane * 8) = f32x4{acc[0][h], acc[1][h], acc[2][h], acc[3][h]};
        *reinterpret_cast<f32x4*>(lds + L_ACC + wave * 256 + h * 128 + lane * 8 + 4) = f32x4{acc[4][h], acc[5][h], acc[6][h], acc[7][h]};
      }
      if (lane < 2) *reinterpret_cast<float2*>(lds + L_ML + (wave * 2 + lane) * 2) = make_float2(m, l);
    }
    LGKM0();
    unsigned old = 0;
    if (lane == 0) old = LDSADD(2);
    old = __builtin_amdgcn_readfirstlane(old);
    if (old == 3u) {
      CFENCE();
      f32x4 mlw[4];
#pragma unroll
      for (int w = 0; w < 4; ++w) mlw[w] = *reinterpret_cast<const f32x4*>(lds + L_ML + w * 4);
      float Mh[2], Lh[2];
#pragma unroll
      for (int h = 0; h < 2; ++h) {
        const float M = fmaxf(fmaxf(mlw[0][2 * h], mlw[1][2 * h]), fmaxf(mlw[2][2 * h], mlw[3][2 * h]));
        float L = 0.f, o0 = 0.f, o1 = 0.f;
#pragma unroll
        for (int w = 0; w < 4; ++w) {
          const float wt = __builtin_amdgcn_exp2f((mlw[w][2 * h] - M) * LOG2E);
          const float2 a = *reinterpret_cast<const float2*>(lds + L_ACC + w * 256 + h * 128 + lane * 2);
          L = fmaf(wt, mlw[w][2 * h + 1], L); o0 = fmaf(wt, a.x, o0); o1 = fmaf(wt, a.y, o1);
        }
        __builtin_amdgcn_raw_buffer_store_b128(u32x4{__float_as_uint(o0), epoch, __float_as_uint(o1), epoch}, rws, WS_PART + ((g * 8 + s) * 256 + h * 128 + lane * 2) * 8, 0, AUX_SC1);
        Mh[h] = M; Lh[h] = L;
      }
      __builtin_amdgcn_raw_buffer_store_b128(u32x4{__float_as_uint(lane ? Mh[1] : Mh[0]), epoch, __float_as_uint(lane ? Lh[1] : Lh[0]), epoch}, rws, lane < 2 ? WS_ML + ((g * 8 + s) * 2 + lane) * 16 : OOB, 0, AUX_SC1);
      if (lane == 0) flag[3] = 1u;
      ST(5);
    }
    STFLUSH(); PTFLUSH();
  } else {
    __builtin_amdgcn_s_setprio(3);
    const int hw = wave;
    const rsrc_t rwq = mkrs(wq, D * NQ * 2), rx = mkrs(x, 2 * D * 2);
    const int cb = s & 3, rq = (s >> 2) + 2 * b;
    const int qrow0 = 144 * rq + 36 * hw + (lane >> 3);
    const unsigned epoch = __builtin_amdgcn_raw_buffer_load_b32(rws, WS_EPOCH, 0, AUX_SC1) + 1u;
    SB();
    u32x3 xr[3];
#pragma unroll
    for (int i = 0; i < 3; ++i) xr[i] = __builtin_amdgcn_raw_buffer_load_b96(rx, lane * 36 + 12 * i, 0, 0);
    u32x4 wqr[5];
#pragma unroll
    for (int i = 0; i < 5; ++i) wqr[i] = __builtin_amdgcn_raw_buffer_load_b128(rwq, (i == 4 && lane >= 32) ? OOB : (qrow0 + 8 * i) * (NQ * 2) + (kvh * 256 + cb * 64 + (lane & 7) * 8) * 2, 0, AUX_NT);
    if (tid < 16) flag[lane] = lane == 8 ? 2u * (2 + NEXTRA) : 0u;
    LGKM0();
    SB();
    __builtin_amdgcn_s_barrier();
    SB();
    ST(1);
    const int gq = lane >> 4, c = lane & 15;
    {
      float v[18];
#pragma unroll
      for (int i = 0; i < 3; ++i)
#pragma unroll
        for (int j = 0; j < 3; ++j) { v[6 * i + 2 * j] = bflo(xr[i][j]); v[6 * i + 2 * j + 1] = bfhi(xr[i][j]); }
      float sm = 0.f;
#pragma unroll
      for (int i = 0; i < 18; ++i) sm += v[i];
      sm += dpp<XOR1>(sm); sm += dpp<XOR2>(sm); sm += dpp<ROR4>(sm); sm += dpp<ROR8>(sm); sm = x16_sum(sm);
      const float mean = sm * (1.f / D);
      float ss = 0.f;
#pragma unroll
      for (int i = 0; i < 18; ++i) { const float d = v[i] - mean; ss = fmaf(d, d, ss); }
      ss += dpp<XOR1>(ss); ss += dpp<XOR2>(ss); ss += dpp<ROR4>(ss); ss += dpp<ROR8>(ss); ss = x16_sum(ss);
      const float mb = rbf(mean), den = rbf(sqrtf(rbf(rbf(ss * (1.f / D)) + 1e-5f))), rden = 1.f / den;
#pragma unroll
      for (int i = 0; i < 18; i += 2) {
        float2 o; o.x = rbf(fdiv(rbf(v[i] - mb), den, rden)); o.y = rbf(fdiv(rbf(v[i + 1] - mb), den, rden));
        *reinterpret_cast<float2*>(lds + L_XN + lane * 18 + i) = o;
      }
      LGKM0();
      float a0[8], a1[8];
#pragma unroll
      for (int j = 0; j < 8; ++j) { a0[j] = 0.f; a1[j] = 0.f; }
#pragma unroll
      for (int i = 0; i < 5; ++i) {
        const int rr = i == 4 ? min(qrow0 + 32, D - 1) : qrow0 + 8 * i;
        const float x0 = lds[L_XN + rr], x1 = lds[L_XN + D + rr];
#pragma unroll
        for (int j = 0; j < 4; ++j) {
          const float lo = bflo(wqr[i][j]), hi = bfhi(wqr[i][j]);
          a0[2 * j] = fmaf(x0, lo, a0[2 * j]); a0[2 * j + 1] = fmaf(x0, hi, a0[2 * j + 1]);
          a1[2 * j] = fmaf(x1, lo, a1[2 * j]); a1[2 * j + 1] = fmaf(x1, hi, a1[2 * j + 1]);
        }
      }
      float r1[8], r2[4];
#pragma unroll
      for (int j = 0; j < 8; ++j) r1[j] = t32_sum(a0[j], a1[j]);
#pragma unroll
      for (int j = 0; j < 4; ++j) { r2[j] = t16_sum(r1[j], r1[4 + j]); r2[j] += dpp<ROR8>(r2[j]); }
      if ((lane & 8) == 0) *reinterpret_cast<f32x4*>(lds + L_QRED + hw * 128 + (lane >> 5) * 64 + (lane & 7) * 8 + ((lane >> 4) & 1) * 4) = f32x4{r2[0], r2[1], r2[2], r2[3]};
      LGKM0();
      if (lane == 0) LDSADD(6);
    }
    ST(2);
    if (hw) {
      const rsrc_t rwg = mkrs(wg, D * F * 2), rwu = mkrs(wu, D * F * 2), rwd = mkrs(wd, F * D * 2), rwo = mkrs(wo, NQ * D * 2);
      const int colA = (hw - 1) * 192 + 8 * c, colB = (hw - 1) * 192 + 128 + 4 * c;
      const int frow0 = 192 * (hw - 1) + (lane >> 2);
      u32x4 gw[12], uw[12], wa[8]; u32x2 wb[8];
#define LDW(WA, WB, rs, row0, T) _Pragma("unroll") for (int j = 0; j < 8; ++j) { \
      VMW((T) - 1); WA[j] = __builtin_amdgcn_raw_buffer_load_b128(rs, (8 * gq + j) * (D * 2) + colA * 2, (row0) * (D * 2), AUX_NT); SB(); \
      VMW((T) - 1); WB[j] = __builtin_amdgcn_raw_buffer_load_b64(rs, (8 * gq + j) * (D * 2) + colB * 2, (row0) * (D * 2), AUX_NT); SB(); } SB()
#define LDGU(u0) _Pragma("unroll") for (int i = 0; i < 12; ++i) { \
      VMW(FFN_T - 1); gw[i] = __builtin_amdgcn_raw_buffer_load_b128(rwg, frow0 * (F * 2) + (lane & 3) * 16, 16 * i * (F * 2) + (u0) * 2, AUX_NT); SB(); \
      VMW(FFN_T - 1); uw[i] = __builtin_amdgcn_raw_buffer_load_b128(rwu, frow0 * (F * 2) + (lane & 3) * 16, 16 * i * (F * 2) + (u0) * 2, AUX_NT); SB(); } SB()
#define YFRAG(FR, WA, WB) do { \
      _Pragma("unroll") for (int cc = 0; cc < 4; ++cc) { \
        FR[2 * cc] = u32x4{perm_lo(WA[1][cc], WA[0][cc]), perm_lo(WA[3][cc], WA[2][cc]), perm_lo(WA[5][cc], WA[4][cc]), perm_lo(WA[7][cc], WA[6][cc])}; \
        FR[2 * cc + 1] = u32x4{perm_hi(WA[1][cc], WA[0][cc]), perm_hi(WA[3][cc], WA[2][cc]), perm_hi(WA[5][cc], WA[4][cc]), perm_hi(WA[7][cc], WA[6][cc])}; } \
      _Pragma("unroll") for (int cc = 0; cc < 2; ++cc) { \
        FR[8 + 2 * cc] = u32x4{perm_lo(WB[1][cc], WB[0][cc]), perm_lo(WB[3][cc], WB[2][cc]), perm_lo(WB[5][cc], WB[4][cc]), perm_lo(WB[7][cc], WB[6][cc])}; \
        FR[9 + 2 * cc] = u32x4{perm_hi(WB[1][cc], WB[0][cc]), perm_hi(WB[3][cc], WB[2][cc]), perm_hi(WB[5][cc], WB[4][cc]), perm_hi(WB[7][cc], WB[6][cc])}; } } while (0)
#define YMFMA(AF, FR) do { \
      f32x4 ya[12]; const f32x4 z4 = {0.f, 0.f, 0.f, 0.f}; \
      _Pragma("unroll") for (int j = 0; j < 12; ++j) ya[j] = mfma(AF, FR[j], z4); \
      const float a0_ = (gq & 1) ? ya[2][0] : ya[0][0], a1_ = (gq & 1) ? ya[6][0] : ya[4][0]; e0 += (gq & 2) ? a1_ : a0_; \
      const float b0_ = (gq & 1) ? ya[3][0] : ya[1][0], b1_ = (gq & 1) ? ya[7][0] : ya[5][0]; e1 += (gq & 2) ? b1_ : b0_; \
      const float c0_ = (gq & 1) ? ya[9][0] : ya[8][0], c1_ = (gq & 1) ? ya[11][0] : ya[10][0]; e2 += (gq & 2) ? c1_ : c0_; } while (0)
      u32x4 fr[12], HA;
      int u0 = 0;
      float e0 = 0.f, e1 = 0.f, e2 = 0.f;
      const int nunit = ((kvh >> 1) == s) ? 2 : 1;
      for (unsigned spin = 0; flag[1] == 0u && spin < (SPIN_CAP << 4); ++spin) __builtin_amdgcn_s_sleep(2);
      __builtin_amdgcn_s_setprio(YPRIO);
#pragma unroll 1
      for (int it = 0; it < nunit; ++it) {
        u0 = (s * 18 + (it == 0 ? kvh : 16 + (kvh & 1))) * 32;
        LDGU(u0);
        float ag[8], au[8];
#pragma unroll
        for (int j = 0; j < 8; ++j) { ag[j] = 0.f; au[j] = 0.f; }
#pragma unroll
        for (int i = 0; i < 12; ++i) {
          const float xv = lds[L_XN + b * D + frow0 + 16 * i];
#pragma unroll
          for (int j = 0; j < 4; ++j) {
            ag[2 * j] = fmaf(xv, bflo(gw[i][j]), ag[2 * j]); ag[2 * j + 1] = fmaf(xv, bfhi(gw[i][j]), ag[2 * j + 1]);
            au[2 * j] = fmaf(xv, bflo(uw[i][j]), au[2 * j]); au[2 * j + 1] = fmaf(xv, bfhi(uw[i][j]), au[2 * j + 1]);
          }
        }
        float r1[8], r2[4];
#pragma unroll
        for (int j = 0; j < 8; ++j) r1[j] = t32_sum(ag[j], au[j]);
#pragma unroll
        for (int j = 0; j < 4; ++j) { r2[j] = t16_sum(r1[j], r1[4 + j]); r2[j] += dpp<ROR4>(r2[j]); r2[j] += dpp<ROR8>(r2[j]); }
        if ((lane & 12) == 0) *reinterpret_cast<f32x4*>(lds + L_GU + (it * 3 + hw - 1) * 64 + (lane >> 5) * 32 + (lane & 3) * 8 + ((lane >> 4) & 1) * 4) = f32x4{r2[0], r2[1], r2[2], r2[3]};
        LGKM0();
        if (lane == 0) LDSADD(4 + it);
        for (unsigned spin = 0; flag[4 + it] < 3u && spin < (SPIN_CAP << 4); ++spin) __builtin_amdgcn_s_sleep(1);
        CFENCE();
        const int cc = lane & 31;
        const float* gu = lds + L_GU + it * 192 + cc;
        const float gt = rbf((gu[0] + gu[64]) + gu[128]), up = rbf((gu[32] + gu[96]) + gu[160]);
        const float y2 = -2.f * 0.7978845608028654f * LOG2E * (gt + 0.044715f * gt * gt * gt);
        const float ge = rbf(gt / (1.f + __builtin_amdgcn_exp2f(y2)));
        reinterpret_cast<__bf16*>(lds + L_H + hw * 16)[cc] = (__bf16)(ge * up);
        LGKM0();
        HA = *reinterpret_cast<const u32x4*>(lds + L_H + hw * 16 + gq * 4);
        if (!(LATE & 1) || it + 1 < nunit) { LDW(wa, wb, rwd, u0, FFN_T); YFRAG(fr, wa, wb); YMFMA(HA, fr); }
        SB();
      }
      ST(4);
      {
        u32x4 wa2[8]; u32x2 wb2[8];
        if (!(LATE & 2)) { LDW(wa2, wb2, rwo, kvh * 256 + s * 32, FFN_T); }
        if (LATE) { for (unsigned spin = 0; flag[9] == 0u && spin < (SPIN_CAP << 4); ++spin) __builtin_amdgcn_s_sleep(4); __builtin_amdgcn_s_setprio(LATEPRIO); }
        if (LATE & 2) { LDW(wa2, wb2, rwo, kvh * 256 + s * 32, 63); }
        if (LATE & 1) { LDW(wa, wb, rwd, u0, 63); }
        __builtin_amdgcn_s_setprio(3);
        if (LATE & 1) { YFRAG(fr, wa, wb); YMFMA(HA, fr); }
        YFRAG(fr, wa2, wb2);
#pragma unroll
        for (int j = 0; j < 12; ++j) asm volatile("" : "+v"(fr[j]));
        asm volatile("" : "+v"(e0), "+v"(e1), "+v"(e2));
      }
      ST(3);
      for (unsigned spin = 0; flag[3] == 0u && spin < (SPIN_CAP << 4); ++spin) __builtin_amdgcn_s_sleep(4);
      ST(5);
      const int sp = (lane >> 1) & 7;
      const int po = WS_PART + ((g * 8 + sp) * 256 + s * 32 + gq * 8 + (lane & 1) * 4) * 8, mo = WS_ML + ((g * 8 + sp) * 2 + (s >> 2)) * 16;
      u32x4 p0, p1, ml;
      bool ok = false;
      for (unsigned spin = 0;; ++spin) {
        CFENCE();
        if (!ok) {
          p0 = __builtin_amdgcn_raw_buffer_load_b128(rws, po, 0, AUX_SC1);
          p1 = __builtin_amdgcn_raw_buffer_load_b128(rws, po + 16, 0, AUX_SC1);
          ml = __builtin_amdgcn_raw_buffer_load_b128(rws, mo, 0, AUX_SC1);
          ok = (((p0[1] ^ epoch) | (p0[3] ^ epoch) | (p1[1] ^ epoch)) | ((p1[3] ^ epoch) | (ml[1] ^ epoch) | (ml[3] ^ epoch))) == 0u;
        }
        if (__all(ok) || spin > SPIN_CAP) break;
      }
      ST(6);
      const float ms = __uint_as_float(ml[0]);
      float M = ms; M = fmaxf(M, dpp<XOR2>(M)); M = fmaxf(M, dpp<ROR4>(M)); M = fmaxf(M, dpp<ROR8>(M));
      const float wt = __builtin_amdgcn_exp2f((ms - M) * LOG2E);
      float v[5] = {wt * __uint_as_float(p0[0]), wt * __uint_as_float(p0[2]), wt * __uint_as_float(p1[0]), wt * __uint_as_float(p1[2]), wt * __uint_as_float(ml[2])};
#pragma unroll
      for (int j = 0; j < 5; ++j) { v[j] += dpp<XOR2>(v[j]); v[j] += dpp<ROR4>(v[j]); v[j] += dpp<ROR8>(v[j]); }
      const float inv = 1.f / v[4];
      const unsigned o0 = pkbf(v[0] * inv, v[1] * inv), o1 = pkbf(v[2] * inv, v[3] * inv);
      const u32x4 OA = {dppu<0xA0>(o0), dppu<0xA0>(o1), dppu<0xF5>(o0), dppu<0xF5>(o1)};
      YMFMA(OA, fr);
      const int prod = kvh * 8 + s;
      __builtin_amdgcn_raw_buffer_store_b128(u32x4{__float_as_uint(e0), epoch, __float_as_uint(e1), epoch}, rws,
                                             WS_Y + ((b * 72 + (colA >> 3)) * 128 + prod) * 64 + gq * 16, 0, AUX_SC1);
      __builtin_amdgcn_raw_buffer_store_b64(u32x2{__float_as_uint(e2), epoch}, rws,
                                            WS_Y + ((b * 72 + (colB >> 3)) * 128 + prod) * 64 + ((colB & 7) + gq) * 8, 0, AUX_SC1);
      if (lane == 0) LDSADD(7);
      ST(7);
    } else {
#define LD8(P, base) do { CFENCE(); _Pragma("unroll") for (int r_ = 0; r_ < 8; ++r_) P[r_] = __builtin_amdgcn_raw_buffer_load_b128(rws, lane * 16 + (r_ & 3) * 1024, (base) + (r_ >> 2) * 4096, AUX_SC1); SB(); } while (0)
#define OK8(P) __all((((P[0][1] ^ epoch) | (P[0][3] ^ epoch) | (P[1][1] ^ epoch) | (P[1][3] ^ epoch)) | ((P[2][1] ^ epoch) | (P[2][3] ^ epoch) | (P[3][1] ^ epoch) | (P[3][3] ^ epoch)) | \
                      ((P[4][1] ^ epoch) | (P[4][3] ^ epoch) | (P[5][1] ^ epoch) | (P[5][3] ^ epoch)) | ((P[6][1] ^ epoch) | (P[6][3] ^ epoch) | (P[7][1] ^ epoch) | (P[7][3] ^ epoch))) == 0u)
#define POLL8(P, base) do { for (unsigned spin = 0;; ++spin) { LD8(P, base); if (OK8(P) || spin > SPIN_CAP) break; } } while (0)
      for (unsigned spin = 0; flag[6] < 4u && spin < (SPIN_CAP << 4); ++spin) {}
      CFENCE();
      {
        const float2 q0 = *reinterpret_cast<const float2*>(lds + L_QRED + 2 * lane), q1 = *reinterpret_cast<const float2*>(lds + L_QRED + 128 + 2 * lane);
        const float2 q2 = *reinterpret_cast<const float2*>(lds + L_QRED + 256 + 2 * lane), q3 = *reinterpret_cast<const float2*>(lds + L_QRED + 384 + 2 * lane);
        const float qa = (q0.x + q1.x) + (q2.x + q3.x), qb = (q0.y + q1.y) + (q2.y + q3.y);
        __builtin_amdgcn_raw_buffer_store_b128(u32x4{__float_as_uint(qa), epoch, __float_as_uint(qb), epoch}, rws,
                                               WS_QP + (((kvh * 2 + (lane >> 5)) * 4 + rq) * 256 + cb * 64 + 2 * (lane & 31)) * 8, 0, AUX_SC1);
      }
      ST(3);
      {
        u32x4 p[8];
        POLL8(p, WS_QP + (kvh * 2 + b) * 8192);
#pragma unroll
        for (int hh = 0; hh < 2; ++hh) {
          const float qa = (__uint_as_float(p[hh][0]) + __uint_as_float(p[2 + hh][0])) + (__uint_as_float(p[4 + hh][0]) + __uint_as_float(p[6 + hh][0]));
          const float qb = (__uint_as_float(p[hh][2]) + __uint_as_float(p[2 + hh][2])) + (__uint_as_float(p[4 + hh][2]) + __uint_as_float(p[6 + hh][2]));
          reinterpret_cast<unsigned*>(lds + L_Q)[hh * 64 + lane] = pkbf(qa, qb);
        }
      }
      if (lane == 0) flag[0] = epoch;
      LGKM0();
      if (lane == 0) flag[1] = 1u;
      ST(4);
      if (s == 0) {
        __builtin_amdgcn_raw_buffer_store_b32(0u, rws, lane == 0 ? WS_CNT + g * 256 : OOB, 0, AUX_SC1);
        asm volatile("s_waitcnt vmcnt(0)" ::: "memory");
        __builtin_amdgcn_raw_buffer_store_b32(epoch, rws, lane == 0 ? WS_CNT + g * 256 + 128 : OOB, 0, AUX_SC1);
      }
      for (unsigned spin = 0; (int)flag[8] < LBASE - 24 && spin < (SPIN_CAP << 2); ++spin) __builtin_amdgcn_s_sleep(32);
      for (unsigned spin = 0;; ++spin) {
        CFENCE();
        const unsigned f = __builtin_amdgcn_raw_buffer_load_b32(rws, WS_CNT + g * 256 + 128, 0, AUX_SC1);
        if (__all(f == epoch) || spin > SPIN_CAP) break;
        __builtin_amdgcn_s_sleep(8);
      }
      {
        const rsrc_t rc = mkrs(ws + WS_CNT + g * 256, 4);
        int req = 0, left = 0;
        for (unsigned spin = 0; spin < (SPIN_CAP << 4); ++spin) {
          int n = min((int)__builtin_amdgcn_readfirstlane((int)flag[8]) - LBASE + LEAD, LMAX) - req;
          if (n <= 0) { __builtin_amdgcn_s_sleep(2); continue; }
          if (GSS) n = min(max(n, left / GSS), LMAX - req);
          const unsigned t0 = (unsigned)__builtin_amdgcn_readfirstlane(__builtin_amdgcn_raw_ptr_buffer_atomic_add_i32(n, rc, lane == 0 ? 0 : 64, 0, 0));
          left = max(NTK - (int)t0 - GSSA * n, 0);
          const bool fin = t0 >= (unsigned)NTK || t0 + (unsigned)n >= (unsigned)NTK || req + n >= LMAX;
          const unsigned tt = t0 + (unsigned)(lane - req);
          if (lane >= req && (fin || lane < req + n)) list[lane] = (lane < req + n && t0 < (unsigned)NTK && tt < (unsigned)NTK) ? tt : 0x7fffffffu;
          req += n;
          if (fin || t0 + (unsigned)(n + LATEREM) >= (unsigned)NTK) { if (lane == 0) flag[9] = 1u; }
          if (fin) break;
        }
      }
      if (blockIdx.x < 144) {
        const int rid = blockIdx.x;
        const unsigned xres = __builtin_amdgcn_raw_buffer_load_b32(rx, rid * 16 + (lane & 3) * 4, 0, 0);
        for (unsigned spin = 0; flag[7] == 0u && spin < (SPIN_CAP << 4); ++spin) __builtin_amdgcn_s_sleep(4);
        ST(5);
        u32x4 y[8];
        {
          int off[8];
#pragma unroll
          for (int i = 0; i < 8; ++i) { off[i] = lane * 16 + i * 1024; y[i] = u32x4{0u, 0u, 0u, 0u}; }
          for (unsigned spin = 0;; ++spin) {
            CFENCE();
            u32x4 z[8];
#pragma unroll
            for (int i = 0; i < 8; ++i) z[i] = __builtin_amdgcn_raw_buffer_load_b128(rws, off[i], WS_Y + rid * 8192, AUX_SC1);
            int all = OOB;
#pragma unroll
            for (int i = 0; i < 8; ++i) { const bool hit = off[i] != OOB && ((z[i][1] ^ epoch) | (z[i][3] ^ epoch)) == 0u;
              y[i][0] = hit ? z[i][0] : y[i][0]; y[i][2] = hit ? z[i][2] : y[i][2]; off[i] = hit ? OOB : off[i]; all &= off[i]; }
            if (__all(all == OOB) || spin > SPIN_CAP) break;
          }
        }
        ST(6);
        float s0 = 0.f, s1 = 0.f;
#pragma unroll
        for (int i = 0; i < 8; ++i) { s0 += __uint_as_float(y[i][0]); s1 += __uint_as_float(y[i][2]); }
        s0 += dpp<ROR4>(s0); s0 += dpp<ROR8>(s0); s0 = x16_sum(s0); s0 = x32_sum(s0);
        s1 += dpp<ROR4>(s1); s1 += dpp<ROR8>(s1); s1 = x16_sum(s1); s1 = x32_sum(s1);
        if (lane < 4) reinterpret_cast<unsigned*>(out)[rid * 4 + lane] = pkbf(bflo(xres) + s0, bfhi(xres) + s1);
        if (rid == 0 && lane == 0) __builtin_amdgcn_raw_buffer_store_b32(epoch, rws, WS_EPOCH, 0, AUX_SC1);
        ST(7);
      }
    }
    STFLUSH();
  }
}
}

extern "C" void kernel_launch(void* const* d_in, const int* in_sizes, int n_in, void* d_out, int out_size, void* d_ws, size_t ws_size, hipStream_t stream) {
  using fk::u16;
  fk::fused<<<256, 512, 0, stream>>>((const u16*)d_in[0], (const u16*)d_in[1], (const u16*)d_in[2], (const u16*)d_in[3], (const u16*)d_in[4],
                                     (const u16*)d_in[6], (const u16*)d_in[7], (const u16*)d_in[8], (u16*)d_out, (unsigned char*)d_ws);
}
```

```cpp
#include <hip/hip_runtime.h>
#include <hip/hip_bf16.h>
#include <stdint.h>

namespace fk {
typedef unsigned short u16;
typedef unsigned u32x4 __attribute__((ext_vector_type(4)));
typedef unsigned u32x2 __attribute__((ext_vector_type(2)));
typedef unsigned u32x3 __attribute__((ext_vector_type(3)));
typedef float f32x4 __attribute__((ext_vector_type(4)));
typedef __bf16 bf16x8 __attribute__((ext_vector_type(8)));
typedef __bf16 bf16x2 __attribute__((ext_vector_type(2)));
#define LAS __attribute__((address_space(3)))
#define SB() __builtin_amdgcn_sched_barrier(0)

constexpr int D = 576, SK = 32768, DH = 128, F = 4608, NQ = 4096;
#define POOL 24
constexpr int CHUNK_B = 8192, PAIR_B = 16384;
constexpr int NP = (128 - POOL) / 2, LBASE = 2 * NP;
constexpr int NTK = 8 * POOL;
constexpr int LMAX = 56;
constexpr int GROUP_B = SK * DH * 2;
static_assert(POOL % 2 == 0 && POOL >= 2 && POOL <= 48, "");
constexpr int WS_EPOCH = 0, WS_CNT = 4096, WS_QP = 16384, WS_PART = WS_QP + 16 * 2 * 4 * 256 * 8, WS_ML = WS_PART + 32 * 8 * 256 * 8,
              WS_Y = WS_ML + 32 * 8 * 2 * 16, WS_END = WS_Y + 144 * 128 * 64, WS_STAMP = 8 << 20;
constexpr int AUX_NT = 2, AUX_SC1 = 16, RSRC3 = 0x00020000, OOB = 0x7f000000;
constexpr unsigned SPIN_CAP = 1u << 18;
constexpr int L_XN = 0;
constexpr int L_QRED = L_XN + 2 * D;
constexpr int L_Q = L_QRED + 4 * 128;
constexpr int L_GU = L_Q + 128;
constexpr int L_H = L_GU + 2 * 3 * 64;
constexpr int L_ACC = L_H + 64;
constexpr int L_ML = L_ACC + 4 * 256;
constexpr int L_FLAG = L_ML + 16;
constexpr int L_DUMMY = L_FLAG + 16;
constexpr int L_LIST = L_DUMMY + 64;
constexpr int L_SC = L_LIST + 64;
constexpr int L_WORDS = L_SC + 65 * 64;
#define NEXTRA 2
#define FFN_T 3
#define LEAD 0
#define PIPEPOLL 0
#define FFN_EARLY 0
#define YPRIO 3
#define CODEPF 0
#define LATE 0
#define LATEREM 0
#define LATEPRIO 0
#define POOL_T 16
#define MAIN_T 16
#define FULLLINE 1
#define MISSONLY 1
#define GSS 12
#define GSSA 6
#define EARLY 8
#define PRE_T 8
#define VMW(n) __builtin_amdgcn_s_waitcnt(0x0F70 | ((n) & 15) | (((n) >> 4) << 14))
static_assert(NEXTRA % 2 == 0, "");

__device__ __forceinline__ float bflo(unsigned v) { return __uint_as_float(v << 16); }
__device__ __forceinline__ float bfhi(unsigned v) { return __uint_as_float(v & 0xffff0000u); }
__device__ __forceinline__ unsigned pkbf(float lo, float hi) { bf16x2 r = {(__bf16)lo, (__bf16)hi}; return __builtin_bit_cast(unsigned, r); }
__device__ __forceinline__ float rbf(float f) { return (float)(__bf16)f; }
__device__ __forceinline__ float fdiv(float a, float d, float r) { const float q = a * r; return fmaf(fmaf(-q, d, a), r, q); }
template <int CTRL> __device__ __forceinline__ float dpp(float x) {
  return __builtin_bit_cast(float, __builtin_amdgcn_mov_dpp(__builtin_bit_cast(int, x), CTRL, 0xf, 0xf, true));
}
template <int CTRL> __device__ __forceinline__ unsigned dppu(unsigned x) { return (unsigned)__builtin_amdgcn_mov_dpp((int)x, CTRL, 0xf, 0xf, true); }
template <int CTRL, int BANK> __device__ __forceinline__ float dpp_bank(float old, float x) {
  return __builtin_bit_cast(float, __builtin_amdgcn_update_dpp(__builtin_bit_cast(int, old), __builtin_bit_cast(int, x), CTRL, 0xf, BANK, false));
}
constexpr int XOR1 = 0xB1, XOR2 = 0x4E, ROR4 = 0x124, ROR8 = 0x128, ROR12 = 0x12C, IDENT = 0xE4;
__device__ __forceinline__ float x16_max(float x) { auto s = __builtin_amdgcn_permlane16_swap(__float_as_uint(x), __float_as_uint(x), false, false); return fmaxf(__uint_as_float(s[0]), __uint_as_float(s[1])); }
__device__ __forceinline__ float x32_max(float x) { auto s = __builtin_amdgcn_permlane32_swap(__float_as_uint(x), __float_as_uint(x), false, false); return fmaxf(__uint_as_float(s[0]), __uint_as_float(s[1])); }
__device__ __forceinline__ float x16_sum(float x) { auto s = __builtin_amdgcn_permlane16_swap(__float_as_uint(x), __float_as_uint(x), false, false); return __uint_as_float(s[0]) + __uint_as_float(s[1]); }
__device__ __forceinline__ float x32_sum(float x) { auto s = __builtin_amdgcn_permlane32_swap(__float_as_uint(x), __float_as_uint(x), false, false); return __uint_as_float(s[0]) + __uint_as_float(s[1]); }
__device__ __forceinline__ float t32_sum(float a, float b) { auto s = __builtin_amdgcn_permlane32_swap(__float_as_uint(a), __float_as_uint(b), false, false); return __uint_as_float(s[0]) + __uint_as_float(s[1]); }
__device__ __forceinline__ float t16_sum(float a, float b) { auto s = __builtin_amdgcn_permlane16_swap(__float_as_uint(a), __float_as_uint(b), false, false); return __uint_as_float(s[0]) + __uint_as_float(s[1]); }
__device__ __forceinline__ f32x4 mfma(u32x4 a, u32x4 b, f32x4 c) {
  return __builtin_amdgcn_mfma_f32_16x16x32_bf16(__builtin_bit_cast(bf16x8, a), __builtin_bit_cast(bf16x8, b), c, 0, 0, 0);
}
__device__ __forceinline__ unsigned perm_lo(unsigned odd, unsigned even) { return __builtin_amdgcn_perm(odd, even, 0x05040100u); }
__device__ __forceinline__ unsigned perm_hi(unsigned odd, unsigned even) { return __builtin_amdgcn_perm(odd, even, 0x07060302u); }
typedef __amdgpu_buffer_rsrc_t rsrc_t;
__device__ __forceinline__ rsrc_t mkrs(const void* p, int bytes) { return __builtin_amdgcn_make_buffer_rsrc((void*)p, 0, bytes, RSRC3); }
#define LDSADD(idx) __hip_atomic_fetch_add((LAS unsigned*)&flag[idx], 1u, __ATOMIC_RELAXED, __HIP_MEMORY_SCOPE_WORKGROUP)
#define LGKM0() asm volatile("s_waitcnt lgkmcnt(0)" ::: "memory")
#define CFENCE() asm volatile("" ::: "memory")

#define PT() do {} while (0)
#define PTFLUSH() do {} while (0)
#define ST(i) do {} while (0)
#define STDECL
#define STFLUSH() do {} while (0)

template <int V> struct IC { static constexpr int v = V; };
#define XG0 28
#define XG1 32
#define XAK 2
#define XAV 0
__global__ void __launch_bounds__(512, 2) fused(const u16* __restrict__ x, const u16* __restrict__ kc, const u16* __restrict__ vc, const u16* __restrict__ wq,
                                                const u16* __restrict__ wo, const u16* __restrict__ wg, const u16* __restrict__ wu, const u16* __restrict__ wd,
                                                u16* out, unsigned char* ws) {
  __shared__ __attribute__((aligned(16))) float lds[L_WORDS];
  volatile LAS unsigned* flag = (volatile LAS unsigned*)(LAS float*)(lds + L_FLAG);
  volatile LAS unsigned* list = (volatile LAS unsigned*)(LAS float*)(lds + L_LIST);
  const int tid = threadIdx.x, lane = tid & 63, wave8 = __builtin_amdgcn_readfirstlane(tid >> 6), wave = wave8 & 3;
  const int g = blockIdx.x >> 3, s = blockIdx.x & 7, b = g >> 4, kvh = g & 15;
  const rsrc_t rws = mkrs(ws, WS_END);
  constexpr float SCALE = 0.088388347648318440f, LOG2E = 1.4426950408889634f;
  const unsigned long long pc0 = __builtin_amdgcn_s_getpc();
  STDECL;
  ST(0);
  if (wave8 >= 4) {
    auto body = [&](auto ak_, auto av_) __attribute__((always_inline)) {
    constexpr int AK = decltype(ak_)::v, AV = decltype(av_)::v;
    const u16* kb = kc + (size_t)g * (SK * DH); const u16* vb = vc + (size_t)g * (SK * DH);
    const int voffK = ((lane >> 2) & 3) * 256 + (lane >> 4) * 64 + (lane & 3) * 16, voffV = lane * 16;
    u32x4 R[2 + NEXTRA][8];
#define LD1A(d, u, rs, vo, so, A) R[d][u] = __builtin_amdgcn_raw_buffer_load_b128(rs, (vo) + ((u) & 3) * 1024, (so) + ((u) >> 2) * 4096, A)
#define LD1(d, u, rs, vo, so) LD1A(d, u, rs, vo, so, AK)
    const rsrc_t rk0 = mkrs(kb, GROUP_B);
#define PREFILL(LO, HI) _Pragma("unroll") for (int i = (LO); i < (HI); ++i) { if (i >= PRE_T) VMW(PRE_T - 1); \
      LD1(i >> 3, i & 7, rk0, voffK, (s << 20) + ((i >> 4) * 4 + wave) * PAIR_B + ((i >> 3) & 1) * CHUNK_B); SB(); }
    PREFILL(0, EARLY)
    {
      const u32x4 sent = {~0u, ~0u, ~0u, ~0u};
#pragma unroll
      for (int i = 0; i < 4; ++i) *reinterpret_cast<u32x4*>(lds + L_SC + i * 1024 + wave * 256 + lane * 4) = sent;
      if (wave == 0) reinterpret_cast<unsigned*>(lds + L_SC)[64 * 64 + lane] = 0xff80ff80u;
      if (wave == 1) reinterpret_cast<unsigned*>(lds + L_LIST)[lane] = ~0u;
      LGKM0();
    }
    __builtin_amdgcn_s_barrier();
    PREFILL(EARLY, 8 * (2 + NEXTRA))
    ST(1);
    for (unsigned spin = 0; flag[1] == 0u && spin < (SPIN_CAP << 4); ++spin) __builtin_amdgcn_s_sleep(1);
    CFENCE();
    const unsigned epoch = flag[0];
    ST(2);
    const int n = lane & 15;
    const u32x4 qv = *reinterpret_cast<const u32x4*>(reinterpret_cast<const u16*>(lds + L_Q) + (n & 1) * 128 + (4 * (lane >> 4) + (n >> 2)) * 8);
    const u32x4 zero4 = {0u, 0u, 0u, 0u};
    const u32x4 Q0 = (n & 2) ? zero4 : qv, Q1 = (n & 2) ? qv : zero4;
    volatile LAS unsigned* scw = (volatile LAS unsigned*)(LAS float*)(lds + L_SC) + lane;
#define QK(d, rs, vo, so, SC, REFILL) QKT(d, rs, vo, so, SC, REFILL, MAIN_T)
#define QKT(d, rs, vo, so, SC, REFILL, T) do { \
      f32x4 dd[4]; \
      _Pragma("unroll") for (int p = 0; p < 4; ++p) { dd[p] = mfma(R[d][2 * p], Q0, f32x4{0.f, 0.f, 0.f, 0.f}); dd[p] = mfma(R[d][2 * p + 1], Q1, dd[p]); SB(); \
        if (REFILL) { if ((T) < 16) VMW((T) - 2); LD1(d, 2 * p, rs, vo, so); SB(); LD1(d, 2 * p + 1, rs, vo, so); SB(); } } \
      float x0 = dpp<IDENT>(dd[0][0]), x1 = dpp<ROR12>(dd[0][1]), x2 = dpp<ROR8>(dd[0][2]), x3 = dpp<ROR4>(dd[0][3]); \
      x0 = dpp_bank<ROR4, 2>(x0, dd[1][0]); x1 = dpp_bank<IDENT, 2>(x1, dd[1][1]); x2 = dpp_bank<ROR12, 2>(x2, dd[1][2]); x3 = dpp_bank<ROR8, 2>(x3, dd[1][3]); \
      x0 = dpp_bank<ROR8, 4>(x0, dd[2][0]); x1 = dpp_bank<ROR4, 4>(x1, dd[2][1]); x2 = dpp_bank<IDENT, 4>(x2, dd[2][2]); x3 = dpp_bank<ROR12, 4>(x3, dd[2][3]); \
      x0 = dpp_bank<ROR12, 8>(x0, dd[3][0]); x1 = dpp_bank<ROR8, 8>(x1, dd[3][1]); x2 = dpp_bank<ROR4, 8>(x2, dd[3][2]); x3 = dpp_bank<IDENT, 8>(x3, dd[3][3]); \
      SC = rbf((x0 + x1) + (x2 + x3)) * SCALE; } while (0)
#define PV(d, rs, vo, so, SC) PVT(d, rs, vo, so, SC, MAIN_T)
#define PVT(d, rs, vo, so, SC, T) do { \
      const float pp = __builtin_amdgcn_exp2f(((SC) - m) * LOG2E); \
      l += pp; \
      const unsigned w_ = pkbf(pp, dpp<0x102>(pp)); \
      const u32x4 PA = {w_, dppu<0x104>(w_), dppu<0x108>(w_), dppu<0x10C>(w_)}; \
      u32x4 bf[8]; \
      SB(); \
      _Pragma("unroll") for (int t_ = 0; t_ < 4; ++t_) { \
        _Pragma("unroll") for (int c = 0; c < 4; ++c) { bf[2 * c][t_] = perm_lo(R[d][2 * t_ + 1][c], R[d][2 * t_][c]); bf[2 * c + 1][t_] = perm_hi(R[d][2 * t_ + 1][c], R[d][2 * t_][c]); } \
        SB(); if ((T) < 16) VMW((T) - 2); LD1A(d, 2 * t_, rs, vo, so, AV); SB(); LD1A(d, 2 * t_ + 1, rs, vo, so, AV); SB(); } \
      _Pragma("unroll") for (int j = 0; j < 8; ++j) acc[j] = mfma(PA, bf[j], acc[j]); \
      SB(); } while (0)
    LAS unsigned* ltp = (LAS unsigned*)(LAS float*)(lds + (lane == 0 ? L_FLAG + 8 : L_DUMMY + lane));
#define LTICKET(inc) __hip_atomic_fetch_add(ltp, inc, __ATOMIC_RELAXED, __HIP_MEMORY_SCOPE_WORKGROUP)
    float m = -3.0e38f;
    int cp = wave, t;
    unsigned lt = LTICKET(1u);
    bool first = true;
    do {
      t = __builtin_amdgcn_readfirstlane((int)lt);
      lt = LTICKET(t < LBASE ? 1u : 0u);
      const bool nk = t < NP;
      const int np = nk ? t : (t < LBASE ? t - NP : 64);
      const rsrc_t rs = mkrs(nk ? kb : vb, GROUP_B);
      const int vo = nk ? voffK : voffV, so = np < 64 ? (s << 20) + np * PAIR_B : GROUP_B;
      SB();
      { float s0, s1; QK(0, rs, vo, so, s0, true); QK(1, rs, vo, so + CHUNK_B, s1, true);
        const unsigned pk = pkbf(s0, s1); scw[cp * 64] = pk; m = fmaxf(fmaxf(m, bflo(pk)), bfhi(pk)); SB(); }
      if (NEXTRA && first) {
#pragma unroll
        for (int d = 2; d < 2 + NEXTRA; d += 2) { float s0, s1; QK(d, rs, 0, 0, s0, false); QK(d + 1, rs, 0, 0, s1, false);
          const unsigned pk = pkbf(s0, s1); scw[((d >> 1) * 4 + wave) * 64] = pk; m = fmaxf(fmaxf(m, bflo(pk)), bfhi(pk)); SB(); PT(); }
        first = false;
      }
      cp = np;
      PT();
    } while (t < NP);
    if (t >= LBASE) lt = (unsigned)t;
    ST(3);
    m = fmaxf(m, dpp<XOR2>(m)); m = fmaxf(m, dpp<ROR4>(m)); m = fmaxf(m, dpp<ROR8>(m)); m = x16_max(m); m = x32_max(m);
    f32x4 acc[8];
#pragma unroll
    for (int j = 0; j < 8; ++j) acc[j] = f32x4{0.f, 0.f, 0.f, 0.f};
    float l = 0.f;
    const rsrc_t rv = mkrs(vb, GROUP_B);
#define RESCALE(SC) do { if (__any((SC) > m)) { \
        float pm = (SC); pm = fmaxf(pm, dpp<XOR2>(pm)); pm = fmaxf(pm, dpp<ROR4>(pm)); pm = fmaxf(pm, dpp<ROR8>(pm)); pm = x16_max(pm); pm = x32_max(pm); \
        const float mn = fmaxf(m, pm), al = __builtin_amdgcn_exp2f((m - mn) * LOG2E); \
        const float al0 = dpp<0x00>(al), al1 = dpp<0x55>(al); \
        l *= al; m = mn; \
        _Pragma("unroll") for (int j = 0; j < 8; ++j) { acc[j][0] *= al0; acc[j][1] *= al1; } } } while (0)
#define POOLITEM(SO) do { const int li = min(t - LBASE, 63); \
        unsigned e = (unsigned)__builtin_amdgcn_readfirstlane((int)list[li]); \
        for (unsigned spin = 0; e == ~0u && spin < (SPIN_CAP << 4); ++spin) e = (unsigned)__builtin_amdgcn_readfirstlane((int)list[li]); \
        SO = e < (unsigned)NTK ? (int)(((e & 7) << 20) + (2 * NP + (e >> 3)) * CHUNK_B) : GROUP_B; } while (0)
    int so;
    do {
      t = __builtin_amdgcn_readfirstlane((int)lt);
      lt = LTICKET(1u);
      unsigned pk = scw[cp * 64];
      const bool nv = t < LBASE;
      so = (s << 20) + (t - NP) * PAIR_B;
      if (!nv) POOLITEM(so);
      const rsrc_t rs0 = mkrs(nv ? vb : kb, GROUP_B);
      const int vo0 = nv ? voffV : voffK, so1 = nv ? so + CHUNK_B : so;
      for (unsigned spin = 0; __any(pk == ~0u) && spin < (SPIN_CAP << 4); ++spin) pk = scw[cp * 64];
      const float sc0 = bflo(pk), sc1 = bfhi(pk), scm = fmaxf(sc0, sc1);
      RESCALE(scm);
      SB();
      PV(0, rs0, vo0, so, sc0);
      PV(1, rv, voffV, so1, sc1);
      cp = t - NP;
      PT();
    } while (t < LBASE);
    ST(6);
    {
      const rsrc_t rk = mkrs(kb, GROUP_B);
      while (so < GROUP_B) {
        t = __builtin_amdgcn_readfirstlane((int)lt);
        lt = LTICKET(1u);
        POOLITEM(so);
        SB();
        float sc;
        QKT(0, rk, voffK, so, sc, true, POOL_T);
        sc = rbf(sc);
        RESCALE(sc);
        PVT(1, rv, voffV, so, sc, POOL_T);
        PT();
      }
    }
    ST(4);
    l += dpp<XOR2>(l); l += dpp<ROR4>(l); l += dpp<ROR8>(l); l = x16_sum(l); l = x32_sum(l);
    if (lane < 16) {
#pragma unroll
      for (int h = 0; h < 2; ++h) {
        *reinterpret_cast<f32x4*>(lds + L_ACC + wave * 256 + h * 128 + lane * 8) = f32x4{acc[0][h], acc[1][h], acc[2][h], acc[3][h]};
        *reinterpret_cast<f32x4*>(lds + L_ACC + wave * 256 + h * 128 + lane * 8 + 4) = f32x4{acc[4][h], acc[5][h], acc[6][h], acc[7][h]};
      }
      if (lane < 2) *reinterpret_cast<float2*>(lds + L_ML + (wave * 2 + lane) * 2) = make_float2(m, l);
    }
    LGKM0();
    unsigned old = 0;
    if (lane == 0) old = LDSADD(2);
    old = __builtin_amdgcn_readfirstlane(old);
    if (old == 3u) {
      CFENCE();
      f32x4 mlw[4];
#pragma unroll
      for (int w = 0; w < 4; ++w) mlw[w] = *reinterpret_cast<const f32x4*>(lds + L_ML + w * 4);
      float Mh[2], Lh[2];
#pragma unroll
      for (int h = 0; h < 2; ++h) {
        const float M = fmaxf(fmaxf(mlw[0][2 * h], mlw[1][2 * h]), fmaxf(mlw[2][2 * h], mlw[3][2 * h]));
        float L = 0.f, o0 = 0.f, o1 = 0.f;
#pragma unroll
        for (int w = 0; w < 4; ++w) {
          const float wt = __builtin_amdgcn_exp2f((mlw[w][2 * h] - M) * LOG2E);
          const float2 a = *reinterpret_cast<const float2*>(lds + L_ACC + w * 256 + h * 128 + lane * 2);
          L = fmaf(wt, mlw[w][2 * h + 1], L); o0 = fmaf(wt, a.x, o0); o1 = fmaf(wt, a.y, o1);
        }
        __builtin_amdgcn_raw_buffer_store_b128(u32x4{__float_as_uint(o0), epoch, __float_as_uint(o1), epoch}, rws, WS_PART + ((g * 8 + s) * 256 + h * 128 + lane * 2) * 8, 0, AUX_SC1);
        Mh[h] = M; Lh[h] = L;
      }
      __builtin_amdgcn_raw_buffer_store_b128(u32x4{__float_as_uint(lane ? Mh[1] : Mh[0]), epoch, __float_as_uint(lane ? Lh[1] : Lh[0]), epoch}, rws, lane < 2 ? WS_ML + ((g * 8 + s) * 2 + lane) * 16 : OOB, 0, AUX_SC1);
      if (lane == 0) flag[3] = 1u;
      ST(5);
    }
    STFLUSH(); PTFLUSH();
    };
    if (g >= XG0 && g < XG1) body(IC<XAK>{}, IC<XAV>{}); else body(IC<2>{}, IC<2>{});
  } else {
    __builtin_amdgcn_s_setprio(3);
    const int hw = wave;
    const rsrc_t rwq = mkrs(wq, D * NQ * 2), rx = mkrs(x, 2 * D * 2);
    const int cb = s & 3, rq = (s >> 2) + 2 * b;
    const int qrow0 = 144 * rq + 36 * hw + (lane >> 3);
    const unsigned epoch = __builtin_amdgcn_raw_buffer_load_b32(rws, WS_EPOCH, 0, AUX_SC1) + 1u;
    SB();
    u32x3 xr[3];
#pragma unroll
    for (int i = 0; i < 3; ++i) xr[i] = __builtin_amdgcn_raw_buffer_load_b96(rx, lane * 36 + 12 * i, 0, 0);
    u32x4 wqr[5];
#pragma unroll
    for (int i = 0; i < 5; ++i) wqr[i] = __builtin_amdgcn_raw_buffer_load_b128(rwq, (i == 4 && lane >= 32) ? OOB : (qrow0 + 8 * i) * (NQ * 2) + (kvh * 256 + cb * 64 + (lane & 7) * 8) * 2, 0, AUX_NT);
    if (tid < 16) flag[lane] = lane == 8 ? 2u * (2 + NEXTRA) : 0u;
    LGKM0();
    SB();
    __builtin_amdgcn_s_barrier();
    SB();
    ST(1);
    const int gq = lane >> 4, c = lane & 15;
    {
      float v[18];
#pragma unroll
      for (int i = 0; i < 3; ++i)
#pragma unroll
        for (int j = 0; j < 3; ++j) { v[6 * i + 2 * j] = bflo(xr[i][j]); v[6 * i + 2 * j + 1] = bfhi(xr[i][j]); }
      float sm = 0.f;
#pragma unroll
      for (int i = 0; i < 18; ++i) sm += v[i];
      sm += dpp<XOR1>(sm); sm += dpp<XOR2>(sm); sm += dpp<ROR4>(sm); sm += dpp<ROR8>(sm); sm = x16_sum(sm);
      const float mean = sm * (1.f / D);
      float ss = 0.f;
#pragma unroll
      for (int i = 0; i < 18; ++i) { const float d = v[i] - mean; ss = fmaf(d, d, ss); }
      ss += dpp<XOR1>(ss); ss += dpp<XOR2>(ss); ss += dpp<ROR4>(ss); ss += dpp<ROR8>(ss); ss = x16_sum(ss);
      const float mb = rbf(mean), den = rbf(sqrtf(rbf(rbf(ss * (1.f / D)) + 1e-5f))), rden = 1.f / den;
#pragma unroll
      for (int i = 0; i < 18; i += 2) {
        float2 o; o.x = rbf(fdiv(rbf(v[i] - mb), den, rden)); o.y = rbf(fdiv(rbf(v[i + 1] - mb), den, rden));
        *reinterpret_cast<float2*>(lds + L_XN + lane * 18 + i) = o;
      }
      LGKM0();
      float a0[8], a1[8];
#pragma unroll
      for (int j = 0; j < 8; ++j) { a0[j] = 0.f; a1[j] = 0.f; }
#pragma unroll
      for (int i = 0; i < 5; ++i) {
        const int rr = i == 4 ? min(qrow0 + 32, D - 1) : qrow0 + 8 * i;
        const float x0 = lds[L_XN + rr], x1 = lds[L_XN + D + rr];
#pragma unroll
        for (int j = 0; j < 4; ++j) {
          const float lo = bflo(wqr[i][j]), hi = bfhi(wqr[i][j]);
          a0[2 * j] = fmaf(x0, lo, a0[2 * j]); a0[2 * j + 1] = fmaf(x0, hi, a0[2 * j + 1]);
          a1[2 * j] = fmaf(x1, lo, a1[2 * j]); a1[2 * j + 1] = fmaf(x1, hi, a1[2 * j + 1]);
        }
      }
      float r1[8], r2[4];
#pragma unroll
      for (int j = 0; j < 8; ++j) r1[j] = t32_sum(a0[j], a1[j]);
#pragma unroll
      for (int j = 0; j < 4; ++j) { r2[j] = t16_sum(r1[j], r1[4 + j]); r2[j] += dpp<ROR8>(r2[j]); }
      if ((lane & 8) == 0) *reinterpret_cast<f32x4*>(lds + L_QRED + hw * 128 + (lane >> 5) * 64 + (lane & 7) * 8 + ((lane >> 4) & 1) * 4) = f32x4{r2[0], r2[1], r2[2], r2[3]};
      LGKM0();
      if (lane == 0) LDSADD(6);
    }
    ST(2);
    if (hw) {
      const rsrc_t rwg = mkrs(wg, D * F * 2), rwu = mkrs(wu, D * F * 2), rwd = mkrs(wd, F * D * 2), rwo = mkrs(wo, NQ * D * 2);
      const int colA = (hw - 1) * 192 + 8 * c, colB = (hw - 1) * 192 + 128 + 4 * c;
      const int frow0 = 192 * (hw - 1) + (lane >> 2);
      u32x4 gw[12], uw[12], wa[8]; u32x2 wb[8];
#define LDW(WA, WB, rs, row0, T) _Pragma("unroll") for (int j = 0; j < 8; ++j) { \
      VMW((T) - 1); WA[j] = __builtin_amdgcn_raw_buffer_load_b128(rs, (8 * gq + j) * (D * 2) + colA * 2, (row0) * (D * 2), AUX_NT); SB(); \
      VMW((T) - 1); WB[j] = __builtin_amdgcn_raw_buffer_load_b64(rs, (8 * gq + j) * (D * 2) + colB * 2, (row0) * (D * 2), AUX_NT); SB(); } SB()
#define LDGU(u0) _Pragma("unroll") for (int i = 0; i < 12; ++i) { \
      VMW(FFN_T - 1); gw[i] = __builtin_amdgcn_raw_buffer_load_b128(rwg, frow0 * (F * 2) + (lane & 3) * 16, 16 * i * (F * 2) + (u0) * 2, AUX_NT); SB(); \
      VMW(FFN_T - 1); uw[i] = __builtin_amdgcn_raw_buffer_load_b128(rwu, frow0 * (F * 2) + (lane & 3) * 16, 16 * i * (F * 2) + (u0) * 2, AUX_NT); SB(); } SB()
#define YFRAG(FR, WA, WB) do { \
      _Pragma("unroll") for (int cc = 0; cc < 4; ++cc) { \
        FR[2 * cc] = u32x4{perm_lo(WA[1][cc], WA[0][cc]), perm_lo(WA[3][cc], WA[2][cc]), perm_lo(WA[5][cc], WA[4][cc]), perm_lo(WA[7][cc], WA[6][cc])}; \
        FR[2 * cc + 1] = u32x4{perm_hi(WA[1][cc], WA[0][cc]), perm_hi(WA[3][cc], WA[2][cc]), perm_hi(WA[5][cc], WA[4][cc]), perm_hi(WA[7][cc], WA[6][cc])}; } \
      _Pragma("unroll") for (int cc = 0; cc < 2; ++cc) { \
        FR[8 + 2 * cc] = u32x4{perm_lo(WB[1][cc], WB[0][cc]), perm_lo(WB[3][cc], WB[2][cc]), perm_lo(WB[5][cc], WB[4][cc]), perm_lo(WB[7][cc], WB[6][cc])}; \
        FR[9 + 2 * cc] = u32x4{perm_hi(WB[1][cc], WB[0][cc]), perm_hi(WB[3][cc], WB[2][cc]), perm_hi(WB[5][cc], WB[4][cc]), perm_hi(WB[7][cc], WB[6][cc])}; } } while (0)
#define YMFMA(AF, FR) do { \
      f32x4 ya[12]; const f32x4 z4 = {0.f, 0.f, 0.f, 0.f}; \
      _Pragma("unroll") for (int j = 0; j < 12; ++j) ya[j] = mfma(AF, FR[j], z4); \
      const float a0_ = (gq & 1) ? ya[2][0] : ya[0][0], a1_ = (gq & 1) ? ya[6][0] : ya[4][0]; e0 += (gq & 2) ? a1_ : a0_; \
      const float b0_ = (gq & 1) ? ya[3][0] : ya[1][0], b1_ = (gq & 1) ? ya[7][0] : ya[5][0]; e1 += (gq & 2) ? b1_ : b0_; \
      const float c0_ = (gq & 1) ? ya[9][0] : ya[8][0], c1_ = (gq & 1) ? ya[11][0] : ya[10][0]; e2 += (gq & 2) ? c1_ : c0_; } while (0)
      u32x4 fr[12], HA;
      int u0 = 0;
      float e0 = 0.f, e1 = 0.f, e2 = 0.f;
      const int nunit = ((kvh >> 1) == s) ? 2 : 1;
      for (unsigned spin = 0; flag[1] == 0u && spin < (SPIN_CAP << 4); ++spin) __builtin_amdgcn_s_sleep(2);
      __builtin_amdgcn_s_setprio(YPRIO);
#pragma unroll 1
      for (int it = 0; it < nunit; ++it) {
        u0 = (s * 18 + (it == 0 ? kvh : 16 + (kvh & 1))) * 32;
        LDGU(u0);
        float ag[8], au[8];
#pragma unroll
        for (int j = 0; j < 8; ++j) { ag[j] = 0.f; au[j] = 0.f; }
#pragma unroll
        for (int i = 0; i < 12; ++i) {
          const float xv = lds[L_XN + b * D + frow0 + 16 * i];
#pragma unroll
          for (int j = 0; j < 4; ++j) {
            ag[2 * j] = fmaf(xv, bflo(gw[i][j]), ag[2 * j]); ag[2 * j + 1] = fmaf(xv, bfhi(gw[i][j]), ag[2 * j + 1]);
            au[2 * j] = fmaf(xv, bflo(uw[i][j]), au[2 * j]); au[2 * j + 1] = fmaf(xv, bfhi(uw[i][j]), au[2 * j + 1]);
          }
        }
        float r1[8], r2[4];
#pragma unroll
        for (int j = 0; j < 8; ++j) r1[j] = t32_sum(ag[j], au[j]);
#pragma unroll
        for (int j = 0; j < 4; ++j) { r2[j] = t16_sum(r1[j], r1[4 + j]); r2[j] += dpp<ROR4>(r2[j]); r2[j] += dpp<ROR8>(r2[j]); }
        if ((lane & 12) == 0) *reinterpret_cast<f32x4*>(lds + L_GU + (it * 3 + hw - 1) * 64 + (lane >> 5) * 32 + (lane & 3) * 8 + ((lane >> 4) & 1) * 4) = f32x4{r2[0], r2[1], r2[2], r2[3]};
        LGKM0();
        if (lane == 0) LDSADD(4 + it);
        for (unsigned spin = 0; flag[4 + it] < 3u && spin < (SPIN_CAP << 4); ++spin) __builtin_amdgcn_s_sleep(1);
        CFENCE();
        const int cc = lane & 31;
        const float* gu = lds + L_GU + it * 192 + cc;
        const float gt = rbf((gu[0] + gu[64]) + gu[128]), up = rbf((gu[32] + gu[96]) + gu[160]);
        const float y2 = -2.f * 0.7978845608028654f * LOG2E * (gt + 0.044715f * gt * gt * gt);
        const float ge = rbf(gt / (1.f + __builtin_amdgcn_exp2f(y2)));
        reinterpret_cast<__bf16*>(lds + L_H + hw * 16)[cc] = (__bf16)(ge * up);
        LGKM0();
        HA = *reinterpret_cast<const u32x4*>(lds + L_H + hw * 16 + gq * 4);
        if (!(LATE & 1) || it + 1 < nunit) { LDW(wa, wb, rwd, u0, FFN_T); YFRAG(fr, wa, wb); YMFMA(HA, fr); }
        SB();
      }
      ST(4);
      {
        u32x4 wa2[8]; u32x2 wb2[8];
        if (!(LATE & 2)) { LDW(wa2, wb2, rwo, kvh * 256 + s * 32, FFN_T); }
        if (LATE) { for (unsigned spin = 0; flag[9] == 0u && spin < (SPIN_CAP << 4); ++spin) __builtin_amdgcn_s_sleep(4); __builtin_amdgcn_s_setprio(LATEPRIO); }
        if (LATE & 2) { LDW(wa2, wb2, rwo, kvh * 256 + s * 32, 63); }
        if (LATE & 1) { LDW(wa, wb, rwd, u0, 63); }
        __builtin_amdgcn_s_setprio(3);
        if (LATE & 1) { YFRAG(fr, wa, wb); YMFMA(HA, fr); }
        YFRAG(fr, wa2, wb2);
#pragma unroll
        for (int j = 0; j < 12; ++j) asm volatile("" : "+v"(fr[j]));
        asm volatile("" : "+v"(e0), "+v"(e1), "+v"(e2));
      }
      ST(3);
      for (unsigned spin = 0; flag[3] == 0u && spin < (SPIN_CAP << 4); ++spin) __builtin_amdgcn_s_sleep(4);
      ST(5);
      const int sp = (lane >> 1) & 7;
      const int po = WS_PART + ((g * 8 + sp) * 256 + s * 32 + gq * 8 + (lane & 1) * 4) * 8, mo = WS_ML + ((g * 8 + sp) * 2 + (s >> 2)) * 16;
      u32x4 p0, p1, ml;
      bool ok = false;
      for (unsigned spin = 0;; ++spin) {
        CFENCE();
        if (!ok) {
          p0 = __builtin_amdgcn_raw_buffer_load_b128(rws, po, 0, AUX_SC1);
          p1 = __builtin_amdgcn_raw_buffer_load_b128(rws, po + 16, 0, AUX_SC1);
          ml = __builtin_amdgcn_raw_buffer_load_b128(rws, mo, 0, AUX_SC1);
          ok = (((p0[1] ^ epoch) | (p0[3] ^ epoch) | (p1[1] ^ epoch)) | ((p1[3] ^ epoch) | (ml[1] ^ epoch) | (ml[3] ^ epoch))) == 0u;
        }
        if (__all(ok) || spin > SPIN_CAP) break;
      }
      ST(6);
      const float ms = __uint_as_float(ml[0]);
      float M = ms; M = fmaxf(M, dpp<XOR2>(M)); M = fmaxf(M, dpp<ROR4>(M)); M = fmaxf(M, dpp<ROR8>(M));
      const float wt = __builtin_amdgcn_exp2f((ms - M) * LOG2E);
      float v[5] = {wt * __uint_as_float(p0[0]), wt * __uint_as_float(p0[2]), wt * __uint_as_float(p1[0]), wt * __uint_as_float(p1[2]), wt * __uint_as_float(ml[2])};
#pragma unroll
      for (int j = 0; j < 5; ++j) { v[j] += dpp<XOR2>(v[j]); v[j] += dpp<ROR4>(v[j]); v[j] += dpp<ROR8>(v[j]); }
      const float inv = 1.f / v[4];
      const unsigned o0 = pkbf(v[0] * inv, v[1] * inv), o1 = pkbf(v[2] * inv, v[3] * inv);
      const u32x4 OA = {dppu<0xA0>(o0), dppu<0xA0>(o1), dppu<0xF5>(o0), dppu<0xF5>(o1)};
      YMFMA(OA, fr);
      const int prod = kvh * 8 + s;
      __builtin_amdgcn_raw_buffer_store_b128(u32x4{__float_as_uint(e0), epoch, __float_as_uint(e1), epoch}, rws,
                                             WS_Y + ((b * 72 + (colA >> 3)) * 128 + prod) * 64 + gq * 16, 0, AUX_SC1);
      __builtin_amdgcn_raw_buffer_store_b64(u32x2{__float_as_uint(e2), epoch}, rws,
                                            WS_Y + ((b * 72 + (colB >> 3)) * 128 + prod) * 64 + ((colB & 7) + gq) * 8, 0, AUX_SC1);
      if (lane == 0) LDSADD(7);
      ST(7);
    } else {
#define LD8(P, base) do { CFENCE(); _Pragma("unroll") for (int r_ = 0; r_ < 8; ++r_) P[r_] = __builtin_amdgcn_raw_buffer_load_b128(rws, lane * 16 + (r_ & 3) * 1024, (base) + (r_ >> 2) * 4096, AUX_SC1); SB(); } while (0)
#define OK8(P) __all((((P[0][1] ^ epoch) | (P[0][3] ^ epoch) | (P[1][1] ^ epoch) | (P[1][3] ^ epoch)) | ((P[2][1] ^ epoch) | (P[2][3] ^ epoch) | (P[3][1] ^ epoch) | (P[3][3] ^ epoch)) | \
                      ((P[4][1] ^ epoch) | (P[4][3] ^ epoch) | (P[5][1] ^ epoch) | (P[5][3] ^ epoch)) | ((P[6][1] ^ epoch) | (P[6][3] ^ epoch) | (P[7][1] ^ epoch) | (P[7][3] ^ epoch))) == 0u)
#define POLL8(P, base) do { for (unsigned spin = 0;; ++spin) { LD8(P, base); if (OK8(P) || spin > SPIN_CAP) break; } } while (0)
      for (unsigned spin = 0; flag[6] < 4u && spin < (SPIN_CAP << 4); ++spin) {}
      CFENCE();
      {
        const float2 q0 = *reinterpret_cast<const float2*>(lds + L_QRED + 2 * lane), q1 = *reinterpret_cast<const float2*>(lds + L_QRED + 128 + 2 * lane);
        const float2 q2 = *reinterpret_cast<const float2*>(lds + L_QRED + 256 + 2 * lane), q3 = *reinterpret_cast<const float2*>(lds + L_QRED + 384 + 2 * lane);
        const float qa = (q0.x + q1.x) + (q2.x + q3.x), qb = (q0.y + q1.y) + (q2.y + q3.y);
        __builtin_amdgcn_raw_buffer_store_b128(u32x4{__float_as_uint(qa), epoch, __float_as_uint(qb), epoch}, rws,
                                               WS_QP + (((kvh * 2 + (lane >> 5)) * 4 + rq) * 256 + cb * 64 + 2 * (lane & 31)) * 8, 0, AUX_SC1);
      }
      ST(3);
      {
        u32x4 p[8];
        POLL8(p, WS_QP + (kvh * 2 + b) * 8192);
#pragma unroll
        for (int hh = 0; hh < 2; ++hh) {
          const float qa = (__uint_as_float(p[hh][0]) + __uint_as_float(p[2 + hh][0])) + (__uint_as_float(p[4 + hh][0]) + __uint_as_float(p[6 + hh][0]));
          const float qb = (__uint_as_float(p[hh][2]) + __uint_as_float(p[2 + hh][2])) + (__uint_as_float(p[4 + hh][2]) + __uint_as_float(p[6 + hh][2]));
          reinterpret_cast<unsigned*>(lds + L_Q)[hh * 64 + lane] = pkbf(qa, qb);
        }
      }
      if (lane == 0) flag[0] = epoch;
      LGKM0();
      if (lane == 0) flag[1] = 1u;
      ST(4);
      if (s == 0) {
        __builtin_amdgcn_raw_buffer_store_b32(0u, rws, lane == 0 ? WS_CNT + g * 256 : OOB, 0, AUX_SC1);
        asm volatile("s_waitcnt vmcnt(0)" ::: "memory");
        __builtin_amdgcn_raw_buffer_store_b32(epoch, rws, lane == 0 ? WS_CNT + g * 256 + 128 : OOB, 0, AUX_SC1);
      }
      for (unsigned spin = 0; (int)flag[8] < LBASE - 24 && spin < (SPIN_CAP << 2); ++spin) __builtin_amdgcn_s_sleep(32);
      for (unsigned spin = 0;; ++spin) {
        CFENCE();
        const unsigned f = __builtin_amdgcn_raw_buffer_load_b32(rws, WS_CNT + g * 256 + 128, 0, AUX_SC1);
        if (__all(f == epoch) || spin > SPIN_CAP) break;
        __builtin_amdgcn_s_sleep(8);
      }
      {
        const rsrc_t rc = mkrs(ws + WS_CNT + g * 256, 4);
        int req = 0, left = 0;
        for (unsigned spin = 0; spin < (SPIN_CAP << 4); ++spin) {
          int n = min((int)__builtin_amdgcn_readfirstlane((int)flag[8]) - LBASE + LEAD, LMAX) - req;
          if (n <= 0) { __builtin_amdgcn_s_sleep(2); continue; }
          if (GSS) n = min(max(n, left / GSS), LMAX - req);
          const unsigned t0 = (unsigned)__builtin_amdgcn_readfirstlane(__builtin_amdgcn_raw_ptr_buffer_atomic_add_i32(n, rc, lane == 0 ? 0 : 64, 0, 0));
          left = max(NTK - (int)t0 - GSSA * n, 0);
          const bool fin = t0 >= (unsigned)NTK || t0 + (unsigned)n >= (unsigned)NTK || req + n >= LMAX;
          const unsigned tt = t0 + (unsigned)(lane - req);
          if (lane >= req && (fin || lane < req + n)) list[lane] = (lane < req + n && t0 < (unsigned)NTK && tt < (unsigned)NTK) ? tt : 0x7fffffffu;
          req += n;
          if (fin || t0 + (unsigned)(n + LATEREM) >= (unsigned)NTK) { if (lane == 0) flag[9] = 1u; }
          if (fin) break;
        }
      }
      if (blockIdx.x < 144) {
        const int rid = blockIdx.x;
        const unsigned xres = __builtin_amdgcn_raw_buffer_load_b32(rx, rid * 16 + (lane & 3) * 4, 0, 0);
        for (unsigned spin = 0; flag[7] == 0u && spin < (SPIN_CAP << 4); ++spin) __builtin_amdgcn_s_sleep(4);
        ST(5);
        u32x4 y[8];
        {
          int off[8];
#pragma unroll
          for (int i = 0; i < 8; ++i) { off[i] = lane * 16 + i * 1024; y[i] = u32x4{0u, 0u, 0u, 0u}; }
          for (unsigned spin = 0;; ++spin) {
            CFENCE();
            u32x4 z[8];
#pragma unroll
            for (int i = 0; i < 8; ++i) z[i] = __builtin_amdgcn_raw_buffer_load_b128(rws, off[i], WS_Y + rid * 8192, AUX_SC1);
            int all = OOB;
#pragma unroll
            for (int i = 0; i < 8; ++i) { const bool hit = off[i] != OOB && ((z[i][1] ^ epoch) | (z[i][3] ^ epoch)) == 0u;
              y[i][0] = hit ? z[i][0] : y[i][0]; y[i][2] = hit ? z[i][2] : y[i][2]; off[i] = hit ? OOB : off[i]; all &= off[i]; }
            if (__all(all == OOB) || spin > SPIN_CAP) break;
          }
        }
        ST(6);
        float s0 = 0.f, s1 = 0.f;
#pragma unroll
        for (int i = 0; i < 8; ++i) { s0 += __uint_as_float(y[i][0]); s1 += __uint_as_float(y[i][2]); }
        s0 += dpp<ROR4>(s0); s0 += dpp<ROR8>(s0); s0 = x16_sum(s0); s0 = x32_sum(s0);
        s1 += dpp<ROR4>(s1); s1 += dpp<ROR8>(s1); s1 = x16_sum(s1); s1 = x32_sum(s1);
        if (lane < 4) reinterpret_cast<unsigned*>(out)[rid * 4 + lane] = pkbf(bflo(xres) + s0, bfhi(xres) + s1);
        if (rid == 0 && lane == 0) __builtin_amdgcn_raw_buffer_store_b32(epoch, rws, WS_EPOCH, 0, AUX_SC1);
        ST(7);
      }
    }
    STFLUSH();
  }
}
}

extern "C" void kernel_launch(void* const* d_in, const int* in_sizes, int n_in, void* d_out, int out_size, void* d_ws, size_t ws_size, hipStream_t stream) {
  using fk::u16;
  fk::fused<<<256, 512, 0, stream>>>((const u16*)d_in[0], (const u16*)d_in[1], (const u16*)d_in[2], (const u16*)d_in[3], (const u16*)d_in[4],
                                     (const u16*)d_in[6], (const u16*)d_in[7], (const u16*)d_in[8], (u16*)d_out, (unsigned char*)d_ws);
}
```

```cpp
#include <hip/hip_runtime.h>
#include <hip/hip_bf16.h>
#include <stdint.h>

namespace fk {
typedef unsigned short u16;
typedef unsigned u32x4 __attribute__((ext_vector_type(4)));
typedef unsigned u32x2 __attribute__((ext_vector_type(2)));
typedef unsigned u32x3 __attribute__((ext_vector_type(3)));
typedef float f32x4 __attribute__((ext_vector_type(4)));
typedef float f32x2 __attribute__((ext_vector_type(2)));
typedef __bf16 bf16x8 __attribute__((ext_vector_type(8)));
typedef __bf16 bf16x2 __attribute__((ext_vector_type(2)));
#define LAS __attribute__((address_space(3)))
#define SB() __builtin_amdgcn_sched_barrier(0)

constexpr int D = 576, SK = 32768, DH = 128, F = 4608, NQ = 4096;
#define POOL 24
#define PH 0
#define NEXTRA 4
#define FFN_T 3
#define PRE_T 12
#define GSS 12
#define GSSA 6
#define QSLEEP 8
#define QSLEEP0 24
#define FFN_EARLY 1
#define CODEPF 18
#define CPFW 4
#define ARGS1 1
#define PRE_N 64
#define PRE_T2 16
#define YSLEEP 0
#define RSLEEP 0
#define LEAD 0
constexpr int CHUNK_B = 8192, PAIR_B = 16384;
constexpr int NP0 = (128 - POOL) / 2;
#define XP 6
constexpr int NFULL = 8 * (POOL - PH), NTK = 2 * XP + NFULL + 16 * PH;
constexpr int LMAX = 96;
constexpr int GROUP_B = SK * DH * 2;
static_assert(POOL % 2 == 0 && POOL >= 2 && PH <= POOL && NTK <= 8 * LMAX && NEXTRA % 2 == 0, "");
constexpr int WS_EPOCH = 0, WS_CNT = 4096, WS_QP = 16384, WS_PART = WS_QP + 16 * 2 * 4 * 256 * 8, WS_ML = WS_PART + 32 * 8 * 256 * 8,
              WS_Y = WS_ML + 32 * 8 * 8 * 16, WS_END = WS_Y + 144 * 128 * 64, WS_STAMP = 8 << 20;
constexpr int AUX_NT = 2, AUX_SC1 = 16, RSRC3 = 0x00020000, OOB = 0x7f000000;
constexpr unsigned SPIN_CAP = 1u << 18;
constexpr int L_XN = 0;
constexpr int L_QRED = L_XN + 2 * D;
constexpr int L_Q = L_QRED + 4 * 128;
constexpr int L_GU = L_Q + 128;
constexpr int L_H = L_GU + 2 * 3 * 64;
constexpr int L_ACC = L_H + 64;
constexpr int L_ML = L_ACC + 4 * 256;
constexpr int L_FLAG = L_ML + 16;
constexpr int L_DUMMY = L_FLAG + 16;
constexpr int L_LIST = L_DUMMY + 64;
constexpr int L_SC = L_LIST + 128;
constexpr int L_WORDS = L_SC + 65 * 64;
#define VMW(n) __builtin_amdgcn_s_waitcnt(0x0F70 | ((n) & 15) | (((n) >> 4) << 14))

__device__ __forceinline__ float bflo(unsigned v) { return __uint_as_float(v << 16); }
__device__ __forceinline__ float bfhi(unsigned v) { return __uint_as_float(v & 0xffff0000u); }
__device__ __forceinline__ unsigned pkbf(float lo, float hi) { bf16x2 r = {(__bf16)lo, (__bf16)hi}; return __builtin_bit_cast(unsigned, r); }
__device__ __forceinline__ float rbf(float f) { return (float)(__bf16)f; }
__device__ __forceinline__ float fdiv(float a, float d, float r) { const float q = a * r; return fmaf(fmaf(-q, d, a), r, q); }
template <int CTRL> __device__ __forceinline__ float dpp(float x) {
  return __builtin_bit_cast(float, __builtin_amdgcn_mov_dpp(__builtin_bit_cast(int, x), CTRL, 0xf, 0xf, true));
}
template <int CTRL> __device__ __forceinline__ unsigned dppu(unsigned x) { return (unsigned)__builtin_amdgcn_mov_dpp((int)x, CTRL, 0xf, 0xf, true); }
template <int CTRL, int BANK> __device__ __forceinline__ float dpp_bank(float old, float x) {
  return __builtin_bit_cast(float, __builtin_amdgcn_update_dpp(__builtin_bit_cast(int, old), __builtin_bit_cast(int, x), CTRL, 0xf, BANK, false));
}
constexpr int XOR1 = 0xB1, XOR2 = 0x4E, ROR4 = 0x124, ROR8 = 0x128, ROR12 = 0x12C, IDENT = 0xE4;
__device__ __forceinline__ float x16_max(float x) { auto s = __builtin_amdgcn_permlane16_swap(__float_as_uint(x), __float_as_uint(x), false, false); return fmaxf(__uint_as_float(s[0]), __uint_as_float(s[1])); }
__device__ __forceinline__ float x32_max(float x) { auto s = __builtin_amdgcn_permlane32_swap(__float_as_uint(x), __float_as_uint(x), false, false); return fmaxf(__uint_as_float(s[0]), __uint_as_float(s[1])); }
__device__ __forceinline__ float x16_sum(float x) { auto s = __builtin_amdgcn_permlane16_swap(__float_as_uint(x), __float_as_uint(x), false, false); return __uint_as_float(s[0]) + __uint_as_float(s[1]); }
__device__ __forceinline__ float x32_sum(float x) { auto s = __builtin_amdgcn_permlane32_swap(__float_as_uint(x), __float_as_uint(x), false, false); return __uint_as_float(s[0]) + __uint_as_float(s[1]); }
__device__ __forceinline__ float t32_sum(float a, float b) { auto s = __builtin_amdgcn_permlane32_swap(__float_as_uint(a), __float_as_uint(b), false, false); return __uint_as_float(s[0]) + __uint_as_float(s[1]); }
__device__ __forceinline__ float t16_sum(float a, float b) { auto s = __builtin_amdgcn_permlane16_swap(__float_as_uint(a), __float_as_uint(b), false, false); return __uint_as_float(s[0]) + __uint_as_float(s[1]); }
__device__ __forceinline__ f32x4 mfma(u32x4 a, u32x4 b, f32x4 c) {
  return __builtin_amdgcn_mfma_f32_16x16x32_bf16(__builtin_bit_cast(bf16x8, a), __builtin_bit_cast(bf16x8, b), c, 0, 0, 0);
}
__device__ __forceinline__ unsigned perm_lo(unsigned odd, unsigned even) { return __builtin_amdgcn_perm(odd, even, 0x05040100u); }
__device__ __forceinline__ unsigned perm_hi(unsigned odd, unsigned even) { return __builtin_amdgcn_perm(odd, even, 0x07060302u); }
typedef __amdgpu_buffer_rsrc_t rsrc_t;
__device__ __forceinline__ rsrc_t mkrs(const void* p, int bytes) { return __builtin_amdgcn_make_buffer_rsrc((void*)p, 0, bytes, RSRC3); }
#define LDSADD(idx, inc) __hip_atomic_fetch_add((LAS unsigned*)(LAS float*)(lds + (lane == 0 ? L_FLAG + (idx) : L_DUMMY + lane)), inc, __ATOMIC_RELAXED, __HIP_MEMORY_SCOPE_WORKGROUP)
#define LGKM0() asm volatile("s_waitcnt lgkmcnt(0)" ::: "memory")
#define CFENCE() asm volatile("" ::: "memory")
#define GRAN2(a, b) u32x4{__float_as_uint(a), epoch, __float_as_uint(b), epoch}

#define ST(i) do {} while (0)
#define STDECL
#define STFLUSH() do {} while (0)
#define PT() do {} while (0)
#define PTFLUSH() do {} while (0)
#define PTR() do {} while (0)

__global__ void __launch_bounds__(512, 2) fused(const u16* __restrict__ x, const u16* __restrict__ kc, const u16* __restrict__ vc, const u16* __restrict__ wq,
                                                const u16* __restrict__ wo, const u16* __restrict__ wg, const u16* __restrict__ wu, const u16* __restrict__ wd,
                                                u16* out, unsigned char* ws) {
  __shared__ __attribute__((aligned(16))) float lds[L_WORDS];
  volatile LAS unsigned* flag = (volatile LAS unsigned*)(LAS float*)(lds + L_FLAG);
  volatile LAS unsigned* list = (volatile LAS unsigned*)(LAS float*)(lds + L_LIST);
  const int tid = threadIdx.x, lane = tid & 63, wave8 = __builtin_amdgcn_readfirstlane(tid >> 6), wave = wave8 & 3;
  const int g = blockIdx.x >> 3, s = blockIdx.x & 7, b = g >> 4, kvh = g & 15;
  const int NP = (kvh >> 1) == s ? NP0 - XP : NP0, LBASE = 2 * NP;
  constexpr float SCALE = 0.088388347648318440f, LOG2E = 1.4426950408889634f;
  unsigned long long pc0;
  asm volatile("s_getpc_b64 %0" : "=s"(pc0));
  const u32x4 cpf = __builtin_amdgcn_raw_buffer_load_b128(mkrs((const void*)(pc0 & ~255ull), CODEPF * 1024), lane * 16, wave8 == CPFW ? g * 1024 : OOB, 0);
  SB();
  asm volatile("" :: "s"(x), "s"(kc), "s"(vc), "s"(wq), "s"(wo), "s"(wg), "s"(wu), "s"(wd), "s"(out), "s"(ws));
  const rsrc_t rws = mkrs(ws, WS_END);
  STDECL;
  ST(0);
  if (wave8 >= 4) {
    const u16* kb = kc + (size_t)g * (SK * DH); const u16* vb = vc + (size_t)g * (SK * DH);
    const int voffK = ((lane >> 2) & 3) * 256 + (lane >> 4) * 64 + (lane & 3) * 16, voffV = lane * 16;
    const rsrc_t rk = mkrs(kb, GROUP_B), rv = mkrs(vb, GROUP_B);
    u32x4 R[2 + NEXTRA][8];
#define LD1(d, u, rs, vo, so, sh) R[d][u] = __builtin_amdgcn_raw_buffer_load_b128(rs, (vo) + ((u) & 3) * 1024, (u) < 4 ? (so) : (sh), AUX_NT)
#define PREFILL(LO, HI) _Pragma("unroll") for (int i = (LO); i < (HI); ++i) { if (i >= PRE_N) { if (i >= PRE_T2) VMW(PRE_T2 - 1); PTR(); } else if (i >= PRE_T) { VMW(PRE_T - 1); PTR(); } \
      const int so_ = (s << 20) + ((i >> 4) * 4 + wave) * PAIR_B + ((i >> 3) & 1) * CHUNK_B; LD1(i >> 3, i & 7, rk, voffK, so_, so_ + 4096); SB(); }
    PREFILL(0, 8)
    {
      const u32x4 sent = {~0u, ~0u, ~0u, ~0u};
#pragma unroll
      for (int i = 0; i < 4; ++i) *reinterpret_cast<u32x4*>(lds + L_SC + i * 1024 + wave * 256 + lane * 4) = sent;
      if (wave == 0) reinterpret_cast<unsigned*>(lds + L_SC)[64 * 64 + lane] = 0xff80ff80u;
      if (wave >= 2) reinterpret_cast<unsigned*>(lds + L_LIST)[(wave - 2) * 64 + lane] = ~0u;
      LGKM0();
    }
    __builtin_amdgcn_s_barrier();
    SB();
    PREFILL(8, 8 * (2 + NEXTRA))
    asm volatile("" :: "v"(cpf));
    ST(1);
    for (unsigned spin = 0; flag[1] == 0u && spin < (SPIN_CAP << 4); ++spin) __builtin_amdgcn_s_sleep(1);
    CFENCE();
    const unsigned epoch = flag[0];
    ST(2);
    const int n = lane & 15;
    const u32x4 qv = *reinterpret_cast<const u32x4*>(reinterpret_cast<const u16*>(lds + L_Q) + (n & 1) * 128 + (4 * (lane >> 4) + (n >> 2)) * 8);
    const u32x4 zero4 = {0u, 0u, 0u, 0u};
    const u32x4 Q0 = (n & 2) ? zero4 : qv, Q1 = (n & 2) ? qv : zero4;
    volatile LAS unsigned* scw = (volatile LAS unsigned*)(LAS float*)(lds + L_SC) + lane;
#define QK(d, rs, vo, so, sh, SC, REFILL) do { \
      f32x4 dd[4]; \
      _Pragma("unroll") for (int p = 0; p < 4; ++p) { dd[p] = mfma(R[d][2 * p], Q0, f32x4{0.f, 0.f, 0.f, 0.f}); dd[p] = mfma(R[d][2 * p + 1], Q1, dd[p]); SB(); \
        if (REFILL) { LD1(d, 2 * p, rs, vo, so, sh); SB(); LD1(d, 2 * p + 1, rs, vo, so, sh); SB(); } } \
      float x0 = dpp<IDENT>(dd[0][0]), x1 = dpp<ROR12>(dd[0][1]), x2 = dpp<ROR8>(dd[0][2]), x3 = dpp<ROR4>(dd[0][3]); \
      x0 = dpp_bank<ROR4, 2>(x0, dd[1][0]); x1 = dpp_bank<IDENT, 2>(x1, dd[1][1]); x2 = dpp_bank<ROR12, 2>(x2, dd[1][2]); x3 = dpp_bank<ROR8, 2>(x3, dd[1][3]); \
      x0 = dpp_bank<ROR8, 4>(x0, dd[2][0]); x1 = dpp_bank<ROR4, 4>(x1, dd[2][1]); x2 = dpp_bank<IDENT, 4>(x2, dd[2][2]); x3 = dpp_bank<ROR12, 4>(x3, dd[2][3]); \
      x0 = dpp_bank<ROR12, 8>(x0, dd[3][0]); x1 = dpp_bank<ROR8, 8>(x1, dd[3][1]); x2 = dpp_bank<ROR4, 8>(x2, dd[3][2]); x3 = dpp_bank<IDENT, 8>(x3, dd[3][3]); \
      SC = rbf((x0 + x1) + (x2 + x3)) * SCALE; } while (0)
#define PV(d, rs, vo, so, sh, SC) do { \
      const float pp = __builtin_amdgcn_exp2f(((SC) - m) * LOG2E); \
      l += pp; \
      const unsigned w_ = pkbf(pp, dpp<0x102>(pp)); \
      const u32x4 PA = {w_, dppu<0x104>(w_), dppu<0x108>(w_), dppu<0x10C>(w_)}; \
      u32x4 bf[8]; \
      SB(); \
      _Pragma("unroll") for (int t_ = 0; t_ < 4; ++t_) { \
        _Pragma("unroll") for (int c = 0; c < 4; ++c) { bf[2 * c][t_] = perm_lo(R[d][2 * t_ + 1][c], R[d][2 * t_][c]); bf[2 * c + 1][t_] = perm_hi(R[d][2 * t_ + 1][c], R[d][2 * t_][c]); } \
        SB(); LD1(d, 2 * t_, rs, vo, so, sh); SB(); LD1(d, 2 * t_ + 1, rs, vo, so, sh); SB(); } \
      _Pragma("unroll") for (int j = 0; j < 8; ++j) acc[j] = mfma(PA, bf[j], acc[j]); \
      SB(); } while (0)
#define RESCALE(SC) do { if (__any((SC) > m)) { \
        float pm = (SC); pm = fmaxf(pm, dpp<XOR2>(pm)); pm = fmaxf(pm, dpp<ROR4>(pm)); pm = fmaxf(pm, dpp<ROR8>(pm)); pm = x16_max(pm); pm = x32_max(pm); \
        const float mn = fmaxf(m, pm), al = __builtin_amdgcn_exp2f((m - mn) * LOG2E); \
        const float al0 = dpp<0x00>(al), al1 = dpp<0x55>(al); \
        l *= al; m = mn; \
        _Pragma("unroll") for (int j = 0; j < 8; ++j) { acc[j][0] *= al0; acc[j][1] *= al1; } } } while (0)
#define POOLITEM(SO, SH) do { const int li = min(t - LBASE, 127); \
        unsigned e = (unsigned)__builtin_amdgcn_readfirstlane((int)list[li]); \
        for (unsigned spin = 0; e == ~0u && spin < (SPIN_CAP << 4); ++spin) e = (unsigned)__builtin_amdgcn_readfirstlane((int)list[li]); \
        SO = e < 8192u ? (int)(e & 4095u) * 4096 : GROUP_B; SH = e < 4096u ? SO + 4096 : GROUP_B; } while (0)
    float m = -3.0e38f;
    int cp = wave, t;
    unsigned lt = LDSADD(8, 1u);
    bool first = true;
    do {
      t = __builtin_amdgcn_readfirstlane((int)lt);
      lt = LDSADD(8, t < LBASE ? 1u : 0u);
      const bool nk = t < NP;
      const int np = nk ? t : (t < LBASE ? t - NP : 64);
      const rsrc_t rs = mkrs(nk ? kb : vb, GROUP_B);
      const int vo = nk ? voffK : voffV, so = np < 64 ? (s << 20) + np * PAIR_B : GROUP_B;
      SB();
      { float s0, s1; QK(0, rs, vo, so, so + 4096, s0, true); QK(1, rs, vo, so + 8192, so + 12288, s1, true);
        const unsigned pk = min(pkbf(s0, s1), 0xfffffffeu); scw[cp * 64] = pk; m = fmaxf(fmaxf(m, bflo(pk)), bfhi(pk)); SB(); }
      if (NEXTRA && first) {
#pragma unroll
        for (int d = 2; d < 2 + NEXTRA; d += 2) { float s0, s1; QK(d, rs, 0, 0, 0, s0, false); QK(d + 1, rs, 0, 0, 0, s1, false);
          const unsigned pk = min(pkbf(s0, s1), 0xfffffffeu); scw[((d >> 1) * 4 + wave) * 64] = pk; m = fmaxf(fmaxf(m, bflo(pk)), bfhi(pk)); SB(); PT(); }
        first = false;
      }
      cp = np;
      PT();
    } while (t < NP);
    if (t >= LBASE) lt = (unsigned)t;
    ST(3);
    m = fmaxf(m, dpp<XOR2>(m)); m = fmaxf(m, dpp<ROR4>(m)); m = fmaxf(m, dpp<ROR8>(m)); m = x16_max(m); m = x32_max(m);
    f32x4 acc[8];
#pragma unroll
    for (int j = 0; j < 8; ++j) acc[j] = f32x4{0.f, 0.f, 0.f, 0.f};
    float l = 0.f;
    int so, sh;
    do {
      t = __builtin_amdgcn_readfirstlane((int)lt);
      lt = LDSADD(8, 1u);
      unsigned pk = scw[cp * 64];
      const bool nv = t < LBASE;
      so = (s << 20) + (t - NP) * PAIR_B; sh = so + 4096;
      if (!nv) POOLITEM(so, sh);
      const rsrc_t rs0 = mkrs(nv ? vb : kb, GROUP_B);
      const int vo0 = nv ? voffV : voffK, add1 = nv ? CHUNK_B : 0;
      for (unsigned spin = 0; __any(pk == ~0u) && spin < (SPIN_CAP << 4); ++spin) pk = scw[cp * 64];
      const float sc0 = bflo(pk), sc1 = bfhi(pk), scm = fmaxf(sc0, sc1);
      RESCALE(scm);
      SB();
      PV(0, rs0, vo0, so, sh, sc0);
      PV(1, rv, voffV, so + add1, sh + add1, sc1);
      cp = t - NP;
      PT();
    } while (t < LBASE);
    ST(6);
    while (so < GROUP_B) {
      const bool dead = sh >= GROUP_B && (lane & 8);
      t = __builtin_amdgcn_readfirstlane((int)lt);
      lt = LDSADD(8, 1u);
      POOLITEM(so, sh);
      SB();
      float sc;
      QK(0, rk, voffK, so, sh, sc, true);
      sc = dead ? -INFINITY : rbf(sc);
      RESCALE(sc);
      PV(1, rv, voffV, so, sh, sc);
      PT();
    }
    ST(4);
    l += dpp<XOR2>(l); l += dpp<ROR4>(l); l += dpp<ROR8>(l); l = x16_sum(l); l = x32_sum(l);
    {
      volatile LAS f32x4* aw = (volatile LAS f32x4*)(LAS float*)(lds + L_ACC + wave * 256 + lane * 8);
      if (lane < 16) {
#pragma unroll
        for (int h = 0; h < 2; ++h) {
          aw[h * 32] = f32x4{acc[0][h], acc[1][h], acc[2][h], acc[3][h]};
          aw[h * 32 + 1] = f32x4{acc[4][h], acc[5][h], acc[6][h], acc[7][h]};
        }
        if (lane < 2) *(volatile LAS f32x2*)(LAS float*)(lds + L_ML + (wave * 2 + lane) * 2) = f32x2{m, l};
      }
    }
    const unsigned old = LDSADD(2, 1u);
    CFENCE();
    f32x2 mlw[4]; f32x4 av[4];
#pragma unroll
    for (int w = 0; w < 4; ++w) {
      mlw[w] = *(volatile LAS f32x2*)(LAS float*)(lds + L_ML + w * 4 + (lane >> 5) * 2);
      av[w] = *(volatile LAS f32x4*)(LAS float*)(lds + L_ACC + w * 256 + lane * 4);
    }
    const int po = WS_PART + (g * 8 + s) * 2048 + lane * 16, mo = (lane & 7) == 0 ? WS_ML + g * 1024 + (lane >> 3) * 128 + s * 16 : OOB;
    if (__builtin_amdgcn_readfirstlane(old) == 3u) {
      const float M = fmaxf(fmaxf(mlw[0][0], mlw[1][0]), fmaxf(mlw[2][0], mlw[3][0]));
      float L = 0.f; f32x4 o = {0.f, 0.f, 0.f, 0.f};
#pragma unroll
      for (int w = 0; w < 4; ++w) {
        const float wt = __builtin_amdgcn_exp2f((mlw[w][0] - M) * LOG2E);
        L = fmaf(wt, mlw[w][1], L); o += wt * av[w];
      }
      __builtin_amdgcn_raw_buffer_store_b128(GRAN2(o[0], o[1]), rws, po, 0, AUX_SC1);
      __builtin_amdgcn_raw_buffer_store_b128(GRAN2(o[2], o[3]), rws, po + 1024, 0, AUX_SC1);
      __builtin_amdgcn_raw_buffer_store_b128(GRAN2(M, L), rws, mo, 0, AUX_SC1);
      flag[3] = 1u;
      ST(5);
    }
    STFLUSH(); PTFLUSH();
  } else {
    __builtin_amdgcn_s_setprio(3);
    const int hw = wave;
    const rsrc_t rwq = mkrs(wq, D * NQ * 2), rx = mkrs(x, 2 * D * 2);
    const int cb = s & 3, rq = (s >> 2) + 2 * b;
    const int qrow0 = 144 * rq + 36 * hw + (lane >> 3);
    const unsigned epoch = __builtin_amdgcn_raw_buffer_load_b32(rws, WS_EPOCH, 0, AUX_SC1) + 1u;
    SB();
    u32x3 xr[3];
#pragma unroll
    for (int i = 0; i < 3; ++i) xr[i] = __builtin_amdgcn_raw_buffer_load_b96(rx, lane * 36 + 12 * i, 0, 0);
    u32x4 wqr[5];
#pragma unroll
    for (int i = 0; i < 5; ++i) wqr[i] = __builtin_amdgcn_raw_buffer_load_b128(rwq, (i == 4 && lane >= 32) ? OOB : (qrow0 + 8 * i) * (NQ * 2) + (kvh * 256 + cb * 64 + (lane & 7) * 8) * 2, 0, AUX_NT);
    if (tid < 16) flag[lane] = lane == 8 ? 2u * (2 + NEXTRA) : 0u;
    LGKM0();
    SB();
    __builtin_amdgcn_s_barrier();
    SB();
    ST(1);
    const int gq = lane >> 4, c = lane & 15;
    {
      float v[18];
#pragma unroll
      for (int i = 0; i < 3; ++i)
#pragma unroll
        for (int j = 0; j < 3; ++j) { v[6 * i + 2 * j] = bflo(xr[i][j]); v[6 * i + 2 * j + 1] = bfhi(xr[i][j]); }
      float sm = 0.f;
#pragma unroll
      for (int i = 0; i < 18; ++i) sm += v[i];
      sm += dpp<XOR1>(sm); sm += dpp<XOR2>(sm); sm += dpp<ROR4>(sm); sm += dpp<ROR8>(sm); sm = x16_sum(sm);
      const float mean = sm * (1.f / D);
      float ss = 0.f;
#pragma unroll
      for (int i = 0; i < 18; ++i) { const float d = v[i] - mean; ss = fmaf(d, d, ss); }
      ss += dpp<XOR1>(ss); ss += dpp<XOR2>(ss); ss += dpp<ROR4>(ss); ss += dpp<ROR8>(ss); ss = x16_sum(ss);
      const float mb = rbf(mean), den = rbf(sqrtf(rbf(rbf(ss * (1.f / D)) + 1e-5f))), rden = 1.f / den;
#pragma unroll
      for (int i = 0; i < 18; i += 2) {
        float2 o; o.x = rbf(fdiv(rbf(v[i] - mb), den, rden)); o.y = rbf(fdiv(rbf(v[i + 1] - mb), den, rden));
        *reinterpret_cast<float2*>(lds + L_XN + lane * 18 + i) = o;
      }
      LGKM0();
      float a0[8], a1[8];
#pragma unroll
      for (int j = 0; j < 8; ++j) { a0[j] = 0.f; a1[j] = 0.f; }
#pragma unroll
      for (int i = 0; i < 5; ++i) {
        const int rr = i == 4 ? min(qrow0 + 32, D - 1) : qrow0 + 8 * i;
        const float x0 = lds[L_XN + rr], x1 = lds[L_XN + D + rr];
#pragma unroll
        for (int j = 0; j < 4; ++j) {
          const float lo = bflo(wqr[i][j]), hi = bfhi(wqr[i][j]);
          a0[2 * j] = fmaf(x0, lo, a0[2 * j]); a0[2 * j + 1] = fmaf(x0, hi, a0[2 * j + 1]);
          a1[2 * j] = fmaf(x1, lo, a1[2 * j]); a1[2 * j + 1] = fmaf(x1, hi, a1[2 * j + 1]);
        }
      }
      float r1[8], r2[4];
#pragma unroll
      for (int j = 0; j < 8; ++j) r1[j] = t32_sum(a0[j], a1[j]);
#pragma unroll
      for (int j = 0; j < 4; ++j) { r2[j] = t16_sum(r1[j], r1[4 + j]); r2[j] += dpp<ROR8>(r2[j]); }
      if ((lane & 8) == 0) *reinterpret_cast<f32x4*>(lds + L_QRED + hw * 128 + (lane >> 5) * 64 + (lane & 7) * 8 + ((lane >> 4) & 1) * 4) = f32x4{r2[0], r2[1], r2[2], r2[3]};
      CFENCE();
      (void)LDSADD(6, 1u);
    }
    ST(2);
    if (hw) {
      const rsrc_t rwg = mkrs(wg, D * F * 2), rwu = mkrs(wu, D * F * 2), rwd = mkrs(wd, F * D * 2), rwo = mkrs(wo, NQ * D * 2);
      const int colA = (hw - 1) * 192 + 8 * c, colB = (hw - 1) * 192 + 128 + 4 * c;
      const int frow0 = 192 * (hw - 1) + (lane >> 2);
      u32x4 gw[12], uw[12], wa[8], fr[12]; u32x2 wb[8];
#define LDW(rs, row0) _Pragma("unroll") for (int j = 0; j < 8; ++j) { \
      VMW(FFN_T - 1); wa[j] = __builtin_amdgcn_raw_buffer_load_b128(rs, (8 * gq + j) * (D * 2) + colA * 2, (row0) * (D * 2), AUX_NT); SB(); \
      VMW(FFN_T - 1); wb[j] = __builtin_amdgcn_raw_buffer_load_b64(rs, (8 * gq + j) * (D * 2) + colB * 2, (row0) * (D * 2), AUX_NT); SB(); } \
      _Pragma("unroll") for (int cc = 0; cc < 4; ++cc) { \
        fr[2 * cc] = u32x4{perm_lo(wa[1][cc], wa[0][cc]), perm_lo(wa[3][cc], wa[2][cc]), perm_lo(wa[5][cc], wa[4][cc]), perm_lo(wa[7][cc], wa[6][cc])}; \
        fr[2 * cc + 1] = u32x4{perm_hi(wa[1][cc], wa[0][cc]), perm_hi(wa[3][cc], wa[2][cc]), perm_hi(wa[5][cc], wa[4][cc]), perm_hi(wa[7][cc], wa[6][cc])}; } \
      _Pragma("unroll") for (int cc = 0; cc < 2; ++cc) { \
        fr[8 + 2 * cc] = u32x4{perm_lo(wb[1][cc], wb[0][cc]), perm_lo(wb[3][cc], wb[2][cc]), perm_lo(wb[5][cc], wb[4][cc]), perm_lo(wb[7][cc], wb[6][cc])}; \
        fr[9 + 2 * cc] = u32x4{perm_hi(wb[1][cc], wb[0][cc]), perm_hi(wb[3][cc], wb[2][cc]), perm_hi(wb[5][cc], wb[4][cc]), perm_hi(wb[7][cc], wb[6][cc])}; }
#define YMFMA(AF) do { \
      f32x4 ya[12]; const f32x4 z4 = {0.f, 0.f, 0.f, 0.f}; \
      _Pragma("unroll") for (int j = 0; j < 12; ++j) ya[j] = mfma(AF, fr[j], z4); \
      const float a0_ = (gq & 1) ? ya[2][0] : ya[0][0], a1_ = (gq & 1) ? ya[6][0] : ya[4][0]; e0 += (gq & 2) ? a1_ : a0_; \
      const float b0_ = (gq & 1) ? ya[3][0] : ya[1][0], b1_ = (gq & 1) ? ya[7][0] : ya[5][0]; e1 += (gq & 2) ? b1_ : b0_; \
      e2 += (gq & 1) ? ya[10][0] : ya[8][0]; e3 += (gq & 1) ? ya[11][0] : ya[9][0]; } while (0)
      float e0 = 0.f, e1 = 0.f, e2 = 0.f, e3 = 0.f;
      const int nunit = ((kvh >> 1) == s) ? 2 : 1;
      if (!FFN_EARLY) { for (unsigned spin = 0; flag[1] == 0u && spin < (SPIN_CAP << 4); ++spin) __builtin_amdgcn_s_sleep(8); }
      if (FFN_EARLY == 2) { for (unsigned spin = 0; (int)flag[8] < 24 && spin < (SPIN_CAP << 4); ++spin) __builtin_amdgcn_s_sleep(8); }
#pragma unroll 1
      for (int it = 0; it < nunit; ++it) {
        const int u0 = (s * 18 + (it == 0 ? kvh : 16 + (kvh & 1))) * 32;
#pragma unroll
        for (int i = 0; i < 12; ++i) {
          VMW(FFN_T - 1); gw[i] = __builtin_amdgcn_raw_buffer_load_b128(rwg, frow0 * (F * 2) + (lane & 3) * 16, 16 * i * (F * 2) + u0 * 2, AUX_NT); SB();
          VMW(FFN_T - 1); uw[i] = __builtin_amdgcn_raw_buffer_load_b128(rwu, frow0 * (F * 2) + (lane & 3) * 16, 16 * i * (F * 2) + u0 * 2, AUX_NT); SB();
        }
        float ag[8], au[8];
#pragma unroll
        for (int j = 0; j < 8; ++j) { ag[j] = 0.f; au[j] = 0.f; }
#pragma unroll
        for (int i = 0; i < 12; ++i) {
          const float xv = lds[L_XN + b * D + frow0 + 16 * i];
#pragma unroll
          for (int j = 0; j < 4; ++j) {
            ag[2 * j] = fmaf(xv, bflo(gw[i][j]), ag[2 * j]); ag[2 * j + 1] = fmaf(xv, bfhi(gw[i][j]), ag[2 * j + 1]);
            au[2 * j] = fmaf(xv, bflo(uw[i][j]), au[2 * j]); au[2 * j + 1] = fmaf(xv, bfhi(uw[i][j]), au[2 * j + 1]);
          }
        }
        float r1[8], r2[4];
#pragma unroll
        for (int j = 0; j < 8; ++j) r1[j] = t32_sum(ag[j], au[j]);
#pragma unroll
        for (int j = 0; j < 4; ++j) { r2[j] = t16_sum(r1[j], r1[4 + j]); r2[j] += dpp<ROR4>(r2[j]); r2[j] += dpp<ROR8>(r2[j]); }
        if ((lane & 12) == 0) *reinterpret_cast<f32x4*>(lds + L_GU + (it * 3 + hw - 1) * 64 + (lane >> 5) * 32 + (lane & 3) * 8 + ((lane >> 4) & 1) * 4) = f32x4{r2[0], r2[1], r2[2], r2[3]};
        CFENCE();
        (void)LDSADD(4 + it, 1u);
        LDW(rwd, u0)
        for (unsigned spin = 0; flag[4 + it] < 3u && spin < (SPIN_CAP << 4); ++spin) __builtin_amdgcn_s_sleep(1);
        CFENCE();
        const int cc = lane & 31;
        const float* gu = lds + L_GU + it * 192 + cc;
        const float gt = rbf((gu[0] + gu[64]) + gu[128]), up = rbf((gu[32] + gu[96]) + gu[160]);
        const float y2 = -2.f * 0.7978845608028654f * LOG2E * (gt + 0.044715f * gt * gt * gt);
        const float ge = rbf(gt / (1.f + __builtin_amdgcn_exp2f(y2)));
        reinterpret_cast<__bf16*>(lds + L_H + hw * 16)[cc] = (__bf16)(ge * up);
        LGKM0();
        const u32x4 HA = *reinterpret_cast<const u32x4*>(lds + L_H + hw * 16 + gq * 4);
        YMFMA(HA);
        SB();
      }
      ST(4);
      LDW(rwo, kvh * 256 + s * 32)
      const int sp = (lane >> 1) & 7;
      int po = WS_PART + (g * 8 + sp) * 2048 + (s * 8 + gq * 2 + (lane & 1)) * 16, mo = WS_ML + g * 1024 + s * 128 + sp * 16;
      int yoA = WS_Y + ((b * 72 + (hw - 1) * 24 + (lane >> 2)) * 128 + kvh * 8 + s) * 64 + (lane & 3) * 16, yoB = lane < 32 ? yoA + 16 * 8192 : OOB;
#pragma unroll
      for (int j = 0; j < 12; ++j) asm volatile("" : "+v"(fr[j]));
      asm volatile("" : "+v"(e0), "+v"(e1), "+v"(e2), "+v"(e3), "+v"(po), "+v"(mo), "+v"(yoA), "+v"(yoB));
      ST(3);
      for (unsigned spin = 0; flag[3] == 0u && spin < (SPIN_CAP << 4); ++spin) __builtin_amdgcn_s_sleep(2);
      ST(5);
      u32x4 p0, p1, ml;
      for (unsigned spin = 0;; ++spin) {
        CFENCE();
        p0 = __builtin_amdgcn_raw_buffer_load_b128(rws, po, 0, AUX_SC1);
        p1 = __builtin_amdgcn_raw_buffer_load_b128(rws, po + 1024, 0, AUX_SC1);
        ml = __builtin_amdgcn_raw_buffer_load_b128(rws, mo, 0, AUX_SC1);
        const bool ok = (((p0[1] ^ epoch) | (p0[3] ^ epoch) | (p1[1] ^ epoch)) | ((p1[3] ^ epoch) | (ml[1] ^ epoch) | (ml[3] ^ epoch))) == 0u;
        if (__all(ok) || spin > SPIN_CAP) break;
        if (YSLEEP) __builtin_amdgcn_s_sleep(YSLEEP);
      }
      ST(6);
      const float ms = __uint_as_float(ml[0]);
      float M = ms; M = fmaxf(M, dpp<XOR2>(M)); M = fmaxf(M, dpp<ROR4>(M)); M = fmaxf(M, dpp<ROR8>(M));
      const float wt = __builtin_amdgcn_exp2f((ms - M) * LOG2E);
      float v[5] = {wt * __uint_as_float(p0[0]), wt * __uint_as_float(p0[2]), wt * __uint_as_float(p1[0]), wt * __uint_as_float(p1[2]), wt * __uint_as_float(ml[2])};
#pragma unroll
      for (int j = 0; j < 5; ++j) { v[j] += dpp<XOR2>(v[j]); v[j] += dpp<ROR4>(v[j]); v[j] += dpp<ROR8>(v[j]); }
      const float r0 = __builtin_amdgcn_rcpf(v[4]), inv = fmaf(fmaf(-v[4], r0, 1.f), r0, r0);
      const unsigned o0 = pkbf(v[0] * inv, v[1] * inv), o1 = pkbf(v[2] * inv, v[3] * inv);
      const u32x4 OA = {dppu<0xA0>(o0), dppu<0xA0>(o1), dppu<0xF5>(o0), dppu<0xF5>(o1)};
      YMFMA(OA);
      __builtin_amdgcn_raw_buffer_store_b128(GRAN2(e0, e1), rws, yoA, 0, AUX_SC1);
      __builtin_amdgcn_raw_buffer_store_b128(GRAN2(e2, e3), rws, yoB, 0, AUX_SC1);
      flag[7] = 1u;
      ST(7);
    } else {
      for (unsigned spin = 0; flag[6] < 4u && spin < (SPIN_CAP << 4); ++spin) {}
      CFENCE();
      {
        const float2 q0 = *reinterpret_cast<const float2*>(lds + L_QRED + 2 * lane), q1 = *reinterpret_cast<const float2*>(lds + L_QRED + 128 + 2 * lane);
        const float2 q2 = *reinterpret_cast<const float2*>(lds + L_QRED + 256 + 2 * lane), q3 = *reinterpret_cast<const float2*>(lds + L_QRED + 384 + 2 * lane);
        const float qa = (q0.x + q1.x) + (q2.x + q3.x), qb = (q0.y + q1.y) + (q2.y + q3.y);
        __builtin_amdgcn_raw_buffer_store_b128(GRAN2(qa, qb), rws, WS_QP + (((kvh * 2 + (lane >> 5)) * 4 + rq) * 256 + cb * 64 + 2 * (lane & 31)) * 8, 0, AUX_SC1);
      }
      ST(3);
#define POLL8(P, base, SLP) do { for (unsigned spin = 0;; ++spin) { CFENCE(); if (SLP) __builtin_amdgcn_s_sleep(SLP); \
        _Pragma("unroll") for (int r_ = 0; r_ < 8; ++r_) P[r_] = __builtin_amdgcn_raw_buffer_load_b128(rws, lane * 16 + (r_ & 3) * 1024, (base) + (r_ >> 2) * 4096, AUX_SC1); \
        unsigned bad_ = 0; _Pragma("unroll") for (int r_ = 0; r_ < 8; ++r_) bad_ |= (P[r_][1] ^ epoch) | (P[r_][3] ^ epoch); \
        if (__all(bad_ == 0u) || spin > SPIN_CAP) break; } } while (0)
      {
        u32x4 p[8];
        if (QSLEEP0) __builtin_amdgcn_s_sleep(QSLEEP0);
        POLL8(p, WS_QP + (kvh * 2 + b) * 8192, QSLEEP);
#pragma unroll
        for (int hh = 0; hh < 2; ++hh) {
          const float qa = (__uint_as_float(p[hh][0]) + __uint_as_float(p[2 + hh][0])) + (__uint_as_float(p[4 + hh][0]) + __uint_as_float(p[6 + hh][0]));
          const float qb = (__uint_as_float(p[hh][2]) + __uint_as_float(p[2 + hh][2])) + (__uint_as_float(p[4 + hh][2]) + __uint_as_float(p[6 + hh][2]));
          reinterpret_cast<unsigned*>(lds + L_Q)[hh * 64 + lane] = pkbf(qa, qb);
        }
      }
      flag[0] = epoch;
      CFENCE();
      flag[1] = 1u;
      ST(4);
      if (s == 0) {
        __builtin_amdgcn_raw_buffer_store_b32(0u, rws, lane == 0 ? WS_CNT + g * 256 : OOB, 0, AUX_SC1);
        asm volatile("s_waitcnt vmcnt(0)" ::: "memory");
        __builtin_amdgcn_raw_buffer_store_b32(epoch, rws, lane == 0 ? WS_CNT + g * 256 + 128 : OOB, 0, AUX_SC1);
      }
      for (unsigned spin = 0; (int)flag[8] < LBASE - 24 && spin < (SPIN_CAP << 2); ++spin) __builtin_amdgcn_s_sleep(32);
      for (unsigned spin = 0;; ++spin) {
        CFENCE();
        const unsigned f = __builtin_amdgcn_raw_buffer_load_b32(rws, WS_CNT + g * 256 + 128, 0, AUX_SC1);
        if (__all(f == epoch) || spin > SPIN_CAP) break;
        __builtin_amdgcn_s_sleep(8);
      }
      {
        const rsrc_t rc = mkrs(ws + WS_CNT + g * 256, 4);
        int req = 0, left = 0;
        for (unsigned spin = 0; spin < (SPIN_CAP << 4) && req < LMAX; ++spin) {
          int nn = (int)__builtin_amdgcn_readfirstlane((int)flag[8]) - LBASE + LEAD - req;
          if (nn <= 0) { __builtin_amdgcn_s_sleep(2); continue; }
          nn = min(max(nn, left / GSS), 24);
          const unsigned t0 = (unsigned)__builtin_amdgcn_readfirstlane(__builtin_amdgcn_raw_ptr_buffer_atomic_add_i32(nn, rc, lane == 0 ? 0 : 64, 0, 0));
          left = max(NTK - (int)t0 - GSSA * nn, 0);
          const int got = t0 >= (unsigned)NTK ? 0 : min(nn, NTK - (int)t0);
          const unsigned tx = t0 + (unsigned)lane, tt = tx - 2 * XP, th = tt - NFULL;
          if (lane < got) list[req + lane] = tx < 2u * XP ? (kvh >> 1) * 256 + (2 * (NP0 - XP) + tx) * 2
                                           : tt < (unsigned)NFULL ? (tt & 7) * 256 + (2 * NP0 + (tt >> 3)) * 2 : 4096 + (th & 7) * 256 + (2 * NP0 + POOL - PH) * 2 + (th >> 3);
          req += got;
          if (got < nn || (int)t0 + nn >= NTK) break;
        }
        if (lane >= req) list[lane] = 0x7fffffffu;
        if (lane + 64 >= req) list[lane + 64] = 0x7fffffffu;
      }
      if (blockIdx.x < 144) {
        const int rid = blockIdx.x, rb = rid % 72, rw = rb / 24, rr = rb % 24;
        const int cc = (rr & 3) * 4 + (lane & 3);
        const int od = (rid / 72) * (D / 2) + rw * 96 + (rr < 16 ? 4 * cc + (rr >> 2) : 64 + 2 * cc + ((rr >> 2) & 1));
        const unsigned xres = __builtin_amdgcn_raw_buffer_load_b32(rx, od * 4, 0, 0);
        for (unsigned spin = 0; flag[7] == 0u && spin < (SPIN_CAP << 4); ++spin) __builtin_amdgcn_s_sleep(2);
        ST(5);
        u32x4 y[8];
        POLL8(y, WS_Y + rid * 8192, RSLEEP);
        ST(6);
        float s0 = 0.f, s1 = 0.f;
#pragma unroll
        for (int i = 0; i < 8; ++i) { s0 += __uint_as_float(y[i][0]); s1 += __uint_as_float(y[i][2]); }
        float st = t32_sum(s0, s1);
        st = x16_sum(st); st += dpp<ROR4>(st); st += dpp<ROR8>(st);
        const auto sw = __builtin_amdgcn_permlane32_swap(__float_as_uint(st), __float_as_uint(st), false, false);
        if (lane < 4) reinterpret_cast<unsigned*>(out)[od] = pkbf(bflo(xres) + __uint_as_float(sw[0]), bfhi(xres) + __uint_as_float(sw[1]));
        if (rid == 0 && lane == 0) __builtin_amdgcn_raw_buffer_store_b32(epoch, rws, WS_EPOCH, 0, AUX_SC1);
        ST(7);
      }
    }
    STFLUSH();
  }
}
}

extern "C" void kernel_launch(void* const* d_in, const int* in_sizes, int n_in, void* d_out, int out_size, void* d_ws, size_t ws_size, hipStream_t stream) {
  using fk::u16;
  fk::fused<<<256, 512, 0, stream>>>((const u16*)d_in[0], (const u16*)d_in[1], (const u16*)d_in[2], (const u16*)d_in[3], (const u16*)d_in[4],
                                     (const u16*)d_in[6], (const u16*)d_in[7], (const u16*)d_in[8], (u16*)d_out, (unsigned char*)d_ws);
}
```

```cpp
#include <hip/hip_runtime.h>
#include <hip/hip_bf16.h>
#include <stdint.h>

namespace fk {
typedef unsigned short u16;
typedef unsigned u32x4 __attribute__((ext_vector_type(4)));
typedef unsigned u32x2 __attribute__((ext_vector_type(2)));
typedef unsigned u32x3 __attribute__((ext_vector_type(3)));
typedef float f32x4 __attribute__((ext_vector_type(4)));
typedef float f32x2 __attribute__((ext_vector_type(2)));
typedef __bf16 bf16x8 __attribute__((ext_vector_type(8)));
typedef __bf16 bf16x2 __attribute__((ext_vector_type(2)));
#define LAS __attribute__((address_space(3)))
#define SB() __builtin_amdgcn_sched_barrier(0)

constexpr int D = 576, SK = 32768, DH = 128, F = 4608, NQ = 4096;
#define POOL 8
#define XK 2
#define NEXTRA 4
#define FFN_T 3
#define PRE_T 12
#define GSS 12
#define GSSA 6
#define QSLEEP 8
#define QSLEEP0 24
#define CODEPF 18
#define PACE 950
#define PACE2 890
#define PACETB 56
#define PACEOFF 200
#define FMAP 0
#define USH 0
#define GAUX AUX_NT
#define UMAP 1
#define LEAD 0
constexpr int CHUNK_B = 8192, PAIR_B = 16384;
constexpr int NP0 = (128 - POOL) / 2;
constexpr int NTK = 8 * POOL;
constexpr int LMAX = 96;
constexpr int GROUP_B = SK * DH * 2;
constexpr int NROW = 72;
static_assert(POOL % 2 == 0 && NTK <= 8 * LMAX && NEXTRA % 2 == 0 && NP0 + XK < NROW && NP0 - 7 * XK >= 2 * (2 + NEXTRA), "");
constexpr int WS_EPOCH = 0, WS_CNT = 4096, WS_QP = 16384, WS_PART = WS_QP + 16 * 2 * 4 * 256 * 8, WS_ML = WS_PART + 32 * 8 * 256 * 8,
              WS_Y = WS_ML + 32 * 8 * 8 * 16, WS_END = WS_Y + 144 * 128 * 64, WS_STAMP = 8 << 20;
constexpr int AUX_NT = 2, AUX_SC1 = 16, RSRC3 = 0x00020000, OOB = 0x7f000000;
constexpr unsigned SPIN_CAP = 1u << 18;
constexpr int L_XN = 0;
constexpr int L_QRED = L_XN + 2 * D;
constexpr int L_Q = L_QRED + 4 * 128;
constexpr int L_GU = L_Q + 128;
constexpr int L_H = L_GU + 2 * 3 * 64;
constexpr int L_ACC = L_H + 64;
constexpr int L_ML = L_ACC + 4 * 256;
constexpr int L_FLAG = L_ML + 16;
constexpr int L_DUMMY = L_FLAG + 16;
constexpr int L_LIST = L_DUMMY + 64;
constexpr int L_SC = L_LIST + 128;
constexpr int L_WORDS = L_SC + NROW * 64;
#define VMW(n) __builtin_amdgcn_s_waitcnt(0x0F70 | ((n) & 15) | (((n) >> 4) << 14))

__device__ __forceinline__ float bflo(unsigned v) { return __uint_as_float(v << 16); }
__device__ __forceinline__ float bfhi(unsigned v) { return __uint_as_float(v & 0xffff0000u); }
__device__ __forceinline__ unsigned pkbf(float lo, float hi) { bf16x2 r = {(__bf16)lo, (__bf16)hi}; return __builtin_bit_cast(unsigned, r); }
__device__ __forceinline__ float rbf(float f) { return (float)(__bf16)f; }
__device__ __forceinline__ float fdiv(float a, float d, float r) { const float q = a * r; return fmaf(fmaf(-q, d, a), r, q); }
template <int CTRL> __device__ __forceinline__ float dpp(float x) {
  return __builtin_bit_cast(float, __builtin_amdgcn_mov_dpp(__builtin_bit_cast(int, x), CTRL, 0xf, 0xf, true));
}
template <int CTRL> __device__ __forceinline__ unsigned dppu(unsigned x) { return (unsigned)__builtin_amdgcn_mov_dpp((int)x, CTRL, 0xf, 0xf, true); }
template <int CTRL, int BANK> __device__ __forceinline__ float dpp_bank(float old, float x) {
  return __builtin_bit_cast(float, __builtin_amdgcn_update_dpp(__builtin_bit_cast(int, old), __builtin_bit_cast(int, x), CTRL, 0xf, BANK, false));
}
constexpr int XOR1 = 0xB1, XOR2 = 0x4E, ROR4 = 0x124, ROR8 = 0x128, ROR12 = 0x12C, IDENT = 0xE4;
__device__ __forceinline__ float x16_max(float x) { auto s = __builtin_amdgcn_permlane16_swap(__float_as_uint(x), __float_as_uint(x), false, false); return fmaxf(__uint_as_float(s[0]), __uint_as_float(s[1])); }
__device__ __forceinline__ float x32_max(float x) { auto s = __builtin_amdgcn_permlane32_swap(__float_as_uint(x), __float_as_uint(x), false, false); return fmaxf(__uint_as_float(s[0]), __uint_as_float(s[1])); }
__device__ __forceinline__ float x16_sum(float x) { auto s = __builtin_amdgcn_permlane16_swap(__float_as_uint(x), __float_as_uint(x), false, false); return __uint_as_float(s[0]) + __uint_as_float(s[1]); }
__device__ __forceinline__ float x32_sum(float x) { auto s = __builtin_amdgcn_permlane32_swap(__float_as_uint(x), __float_as_uint(x), false, false); return __uint_as_float(s[0]) + __uint_as_float(s[1]); }
__device__ __forceinline__ float t32_sum(float a, float b) { auto s = __builtin_amdgcn_permlane32_swap(__float_as_uint(a), __float_as_uint(b), false, false); return __uint_as_float(s[0]) + __uint_as_float(s[1]); }
__device__ __forceinline__ float t16_sum(float a, float b) { auto s = __builtin_amdgcn_permlane16_swap(__float_as_uint(a), __float_as_uint(b), false, false); return __uint_as_float(s[0]) + __uint_as_float(s[1]); }
__device__ __forceinline__ f32x4 mfma(u32x4 a, u32x4 b, f32x4 c) {
  return __builtin_amdgcn_mfma_f32_16x16x32_bf16(__builtin_bit_cast(bf16x8, a), __builtin_bit_cast(bf16x8, b), c, 0, 0, 0);
}
__device__ __forceinline__ unsigned perm_lo(unsigned odd, unsigned even) { return __builtin_amdgcn_perm(odd, even, 0x05040100u); }
__device__ __forceinline__ unsigned perm_hi(unsigned odd, unsigned even) { return __builtin_amdgcn_perm(odd, even, 0x07060302u); }
typedef __amdgpu_buffer_rsrc_t rsrc_t;
__device__ __forceinline__ rsrc_t mkrs(const void* p, int bytes) { return __builtin_amdgcn_make_buffer_rsrc((void*)p, 0, bytes, RSRC3); }
#define LDSADD(idx, inc) __hip_atomic_fetch_add((LAS unsigned*)(LAS float*)(lds + (lane == 0 ? L_FLAG + (idx) : L_DUMMY + lane)), inc, __ATOMIC_RELAXED, __HIP_MEMORY_SCOPE_WORKGROUP)
#define LGKM0() asm volatile("s_waitcnt lgkmcnt(0)" ::: "memory")
#define CFENCE() asm volatile("" ::: "memory")
#define GRAN2(a, b) u32x4{__float_as_uint(a), epoch, __float_as_uint(b), epoch}

#define ST(i) do {} while (0)
#define STDECL
#define STFLUSH() do {} while (0)
#define PT() do {} while (0)
#define PTFLUSH() do {} while (0)
#define PTR() do {} while (0)

__global__ void __launch_bounds__(512, 2) fused(const u16* __restrict__ x, const u16* __restrict__ kc, const u16* __restrict__ vc, const u16* __restrict__ wq,
                                                const u16* __restrict__ wo, const u16* __restrict__ wg, const u16* __restrict__ wu, const u16* __restrict__ wd,
                                                u16* out, unsigned char* ws) {
  __shared__ __attribute__((aligned(16))) float lds[L_WORDS];
  volatile LAS unsigned* flag = (volatile LAS unsigned*)(LAS float*)(lds + L_FLAG);
  volatile LAS unsigned* list = (volatile LAS unsigned*)(LAS float*)(lds + L_LIST);
  const int tid = threadIdx.x, lane = tid & 63, wave8 = __builtin_amdgcn_readfirstlane(tid >> 6), wave = wave8 & 3;
  const int g = blockIdx.x >> 3, s = blockIdx.x & 7, b = g >> 4, kvh = g & 15;
  const unsigned tstart = (unsigned)__builtin_amdgcn_s_memrealtime();
  const int s2 = kvh >> 1;
  const int NP = s == s2 ? NP0 - 7 * XK : NP0 + XK, LBASE = 2 * NP;
  const int tsc = s == s2 ? (128 * 256) / (2 * (NP0 - 7 * XK) + POOL) : (128 * 256) / (2 * (NP0 + XK) + POOL);
#define DUE1(T) (PACEOFF + ((min((T), PACETB) * PACE + max((T) - PACETB, 0) * PACE2) >> 4))
#define DUE(T) DUE1(((T) * tsc) >> 8)
#define RNG(S) ((S) ^ (g >> 2))
  const int obase = RNG(s) << 20, xbase = (RNG(s2) << 20) + ((s - (s > s2)) * XK - 7 * XK) * PAIR_B;
#define POFF(p) (((p) < NP0 ? obase : xbase) + (p) * PAIR_B)
  constexpr float SCALE = 0.088388347648318440f, LOG2E = 1.4426950408889634f;
  unsigned long long pc0;
  asm volatile("s_getpc_b64 %0" : "=s"(pc0));
  const u32x4 cpf = __builtin_amdgcn_raw_buffer_load_b128(mkrs((const void*)(pc0 & ~255ull), CODEPF * 1024), lane * 16, wave8 == 4 ? g * 1024 : OOB, 0);
  SB();
  asm volatile("" :: "s"(x), "s"(kc), "s"(vc), "s"(wq), "s"(wo), "s"(wg), "s"(wu), "s"(wd), "s"(out), "s"(ws));
  const rsrc_t rws = mkrs(ws, WS_END);
  STDECL;
  ST(0);
  if (wave8 >= 4) {
    const u16* kb = kc + (size_t)g * (SK * DH); const u16* vb = vc + (size_t)g * (SK * DH);
    const int voffK = ((lane >> 2) & 3) * 256 + (lane >> 4) * 64 + (lane & 3) * 16, voffV = lane * 16;
    const rsrc_t rk = mkrs(kb, GROUP_B), rv = mkrs(vb, GROUP_B);
    u32x4 R[2 + NEXTRA][8];
#define LD1(d, u, rs, vo, so, sh) R[d][u] = __builtin_amdgcn_raw_buffer_load_b128(rs, (vo) + ((u) & 3) * 1024, (u) < 4 ? (so) : (sh), AUX_NT)
#define PREFILL(LO, HI) _Pragma("unroll") for (int i = (LO); i < (HI); ++i) { if (i >= PRE_T) { VMW(PRE_T - 1); PTR(); } \
      const int so_ = POFF((i >> 4) * 4 + wave) + ((i >> 3) & 1) * CHUNK_B; LD1(i >> 3, i & 7, rk, voffK, so_, so_ + 4096); SB(); }
#define PACEWAIT(T) do { if (PACE) { const unsigned due_ = (unsigned)DUE(T); for (unsigned spin = 0; (unsigned)__builtin_amdgcn_s_memrealtime() - tstart < due_ && spin < 4096u; ++spin) __builtin_amdgcn_s_sleep(2); } } while (0)
    PREFILL(0, 8)
    {
      const u32x4 sent = {~0u, ~0u, ~0u, ~0u};
#pragma unroll
      for (int i = 0; i < 4; ++i) *reinterpret_cast<u32x4*>(lds + L_SC + i * 1024 + wave * 256 + lane * 4) = sent;
      const u32x4 ninf = {0xff80ff80u, 0xff80ff80u, 0xff80ff80u, 0xff80ff80u};
      if (wave < 2) *reinterpret_cast<u32x4*>(lds + L_SC + 4096 + wave * 256 + lane * 4) = (wave == 1 && lane >= 48) ? ninf : sent;
      if (wave >= 2) reinterpret_cast<unsigned*>(lds + L_LIST)[(wave - 2) * 64 + lane] = ~0u;
      LGKM0();
    }
    __builtin_amdgcn_s_barrier();
    SB();
    PREFILL(8, 8 * (2 + NEXTRA))
    asm volatile("" :: "v"(cpf));
    ST(1);
    for (unsigned spin = 0; flag[1] == 0u && spin < (SPIN_CAP << 4); ++spin) __builtin_amdgcn_s_sleep(1);
    CFENCE();
    const unsigned epoch = flag[0];
    ST(2);
    const int n = lane & 15;
    const u32x4 qv = *reinterpret_cast<const u32x4*>(reinterpret_cast<const u16*>(lds + L_Q) + (n & 1) * 128 + (4 * (lane >> 4) + (n >> 2)) * 8);
    const u32x4 zero4 = {0u, 0u, 0u, 0u};
    const u32x4 Q0 = (n & 2) ? zero4 : qv, Q1 = (n & 2) ? qv : zero4;
    volatile LAS unsigned* scw = (volatile LAS unsigned*)(LAS float*)(lds + L_SC) + lane;
#define QK(d, rs, vo, so, sh, SC, REFILL) do { \
      f32x4 dd[4]; \
      _Pragma("unroll") for (int p = 0; p < 4; ++p) { dd[p] = mfma(R[d][2 * p], Q0, f32x4{0.f, 0.f, 0.f, 0.f}); dd[p] = mfma(R[d][2 * p + 1], Q1, dd[p]); SB(); \
        if (REFILL) { LD1(d, 2 * p, rs, vo, so, sh); SB(); LD1(d, 2 * p + 1, rs, vo, so, sh); SB(); } } \
      float x0 = dpp<IDENT>(dd[0][0]), x1 = dpp<ROR12>(dd[0][1]), x2 = dpp<ROR8>(dd[0][2]), x3 = dpp<ROR4>(dd[0][3]); \
      x0 = dpp_bank<ROR4, 2>(x0, dd[1][0]); x1 = dpp_bank<IDENT, 2>(x1, dd[1][1]); x2 = dpp_bank<ROR12, 2>(x2, dd[1][2]); x3 = dpp_bank<ROR8, 2>(x3, dd[1][3]); \
      x0 = dpp_bank<ROR8, 4>(x0, dd[2][0]); x1 = dpp_bank<ROR4, 4>(x1, dd[2][1]); x2 = dpp_bank<IDENT, 4>(x2, dd[2][2]); x3 = dpp_bank<ROR12, 4>(x3, dd[2][3]); \
      x0 = dpp_bank<ROR12, 8>(x0, dd[3][0]); x1 = dpp_bank<ROR8, 8>(x1, dd[3][1]); x2 = dpp_bank<ROR4, 8>(x2, dd[3][2]); x3 = dpp_bank<IDENT, 8>(x3, dd[3][3]); \
      SC = rbf((x0 + x1) + (x2 + x3)) * SCALE; } while (0)
#define PV(d, rs, vo, so, sh, SC) do { \
      const float pp = __builtin_amdgcn_exp2f(((SC) - m) * LOG2E); \
      l += pp; \
      const unsigned w_ = pkbf(pp, dpp<0x102>(pp)); \
      const u32x4 PA = {w_, dppu<0x104>(w_), dppu<0x108>(w_), dppu<0x10C>(w_)}; \
      u32x4 bf[8]; \
      SB(); \
      _Pragma("unroll") for (int t_ = 0; t_ < 4; ++t_) { \
        _Pragma("unroll") for (int c = 0; c < 4; ++c) { bf[2 * c][t_] = perm_lo(R[d][2 * t_ + 1][c], R[d][2 * t_][c]); bf[2 * c + 1][t_] = perm_hi(R[d][2 * t_ + 1][c], R[d][2 * t_][c]); } \
        SB(); LD1(d, 2 * t_, rs, vo, so, sh); SB(); LD1(d, 2 * t_ + 1, rs, vo, so, sh); SB(); } \
      _Pragma("unroll") for (int j = 0; j < 8; ++j) acc[j] = mfma(PA, bf[j], acc[j]); \
      SB(); } while (0)
#define RESCALE(SC) do { if (__any((SC) > m)) { \
        float pm = (SC); pm = fmaxf(pm, dpp<XOR2>(pm)); pm = fmaxf(pm, dpp<ROR4>(pm)); pm = fmaxf(pm, dpp<ROR8>(pm)); pm = x16_max(pm); pm = x32_max(pm); \
        const float mn = fmaxf(m, pm), al = __builtin_amdgcn_exp2f((m - mn) * LOG2E); \
        const float al0 = dpp<0x00>(al), al1 = dpp<0x55>(al); \
        l *= al; m = mn; \
        _Pragma("unroll") for (int j = 0; j < 8; ++j) { acc[j][0] *= al0; acc[j][1] *= al1; } } } while (0)
#define POOLITEM(SO) do { if (POOL) { const int li = min(t - LBASE, 127); \
        unsigned e = (unsigned)__builtin_amdgcn_readfirstlane((int)list[li]); \
        for (unsigned spin = 0; e == ~0u && spin < (SPIN_CAP << 4); ++spin) e = (unsigned)__builtin_amdgcn_readfirstlane((int)list[li]); \
        SO = e < 1024u ? (int)e * CHUNK_B : GROUP_B; } else SO = GROUP_B; } while (0)
    float m = -3.0e38f;
    int cp = wave, t;
    unsigned lt = LDSADD(8, 1u);
    bool first = true;
    do {
      t = __builtin_amdgcn_readfirstlane((int)lt);
      lt = LDSADD(8, t < LBASE ? 1u : 0u);
      const bool nk = t < NP;
      const int np = nk ? t : (t < LBASE ? t - NP : NROW - 1);
      const rsrc_t rs = mkrs(nk ? kb : vb, GROUP_B);
      const int vo = nk ? voffK : voffV, so = np < NROW - 1 ? POFF(np) : GROUP_B;
      PACEWAIT(t);
      SB();
      { float s0, s1; QK(0, rs, vo, so, so + 4096, s0, true); QK(1, rs, vo, so + 8192, so + 12288, s1, true);
        const unsigned pk = min(pkbf(s0, s1), 0xfffffffeu); scw[cp * 64] = pk; m = fmaxf(fmaxf(m, bflo(pk)), bfhi(pk)); SB(); }
      if (NEXTRA && first) {
#pragma unroll
        for (int d = 2; d < 2 + NEXTRA; d += 2) { float s0, s1; QK(d, rs, 0, 0, 0, s0, false); QK(d + 1, rs, 0, 0, 0, s1, false);
          const unsigned pk = min(pkbf(s0, s1), 0xfffffffeu); scw[((d >> 1) * 4 + wave) * 64] = pk; m = fmaxf(fmaxf(m, bflo(pk)), bfhi(pk)); SB(); PT(); }
        first = false;
      }
      cp = np;
      PT();
    } while (t < NP);
    if (t >= LBASE) lt = (unsigned)t;
    ST(3);
    m = fmaxf(m, dpp<XOR2>(m)); m = fmaxf(m, dpp<ROR4>(m)); m = fmaxf(m, dpp<ROR8>(m)); m = x16_max(m); m = x32_max(m);
    f32x4 acc[8];
#pragma unroll
    for (int j = 0; j < 8; ++j) acc[j] = f32x4{0.f, 0.f, 0.f, 0.f};
    float l = 0.f;
    int so;
    do {
      t = __builtin_amdgcn_readfirstlane((int)lt);
      lt = LDSADD(8, 1u);
      unsigned pk = scw[cp * 64];
      const bool nv = t < LBASE;
      so = POFF(t - NP);
      if (!nv) POOLITEM(so);
      const rsrc_t rs0 = mkrs(nv ? vb : kb, GROUP_B);
      const int vo0 = nv ? voffV : voffK, add1 = nv ? CHUNK_B : 0;
      for (unsigned spin = 0; __any(pk == ~0u) && spin < (SPIN_CAP << 4); ++spin) pk = scw[cp * 64];
      const float sc0 = bflo(pk), sc1 = bfhi(pk), scm = fmaxf(sc0, sc1);
      RESCALE(scm);
      PACEWAIT(t);
      SB();
      PV(0, rs0, vo0, so, so + 4096, sc0);
      PV(1, rv, voffV, so + add1, so + add1 + 4096, sc1);
      cp = t - NP;
      PT();
    } while (t < LBASE);
    ST(6);
    while (POOL && so < GROUP_B) {
      t = __builtin_amdgcn_readfirstlane((int)lt);
      lt = LDSADD(8, 1u);
      POOLITEM(so);
      SB();
      float sc;
      QK(0, rk, voffK, so, so + 4096, sc, true);
      sc = rbf(sc);
      RESCALE(sc);
      PV(1, rv, voffV, so, so + 4096, sc);
      PT();
    }
    ST(4);
    l += dpp<XOR2>(l); l += dpp<ROR4>(l); l += dpp<ROR8>(l); l = x16_sum(l); l = x32_sum(l);
    {
      volatile LAS f32x4* aw = (volatile LAS f32x4*)(LAS float*)(lds + L_ACC + wave * 256 + lane * 8);
      if (lane < 16) {
#pragma unroll
        for (int h = 0; h < 2; ++h) {
          aw[h * 32] = f32x4{acc[0][h], acc[1][h], acc[2][h], acc[3][h]};
          aw[h * 32 + 1] = f32x4{acc[4][h], acc[5][h], acc[6][h], acc[7][h]};
        }
        if (lane < 2) *(volatile LAS f32x2*)(LAS float*)(lds + L_ML + (wave * 2 + lane) * 2) = f32x2{m, l};
      }
    }
    const unsigned old = LDSADD(2, 1u);
    CFENCE();
    f32x2 mlw[4]; f32x4 av[4];
#pragma unroll
    for (int w = 0; w < 4; ++w) {
      mlw[w] = *(volatile LAS f32x2*)(LAS float*)(lds + L_ML + w * 4 + (lane >> 5) * 2);
      av[w] = *(volatile LAS f32x4*)(LAS float*)(lds + L_ACC + w * 256 + lane * 4);
    }
    const int po = WS_PART + (g * 8 + s) * 2048 + lane * 16, mo = (lane & 7) == 0 ? WS_ML + g * 1024 + (lane >> 3) * 128 + s * 16 : OOB;
    if (__builtin_amdgcn_readfirstlane(old) == 3u) {
      const float M = fmaxf(fmaxf(mlw[0][0], mlw[1][0]), fmaxf(mlw[2][0], mlw[3][0]));
      float L = 0.f; f32x4 o = {0.f, 0.f, 0.f, 0.f};
#pragma unroll
      for (int w = 0; w < 4; ++w) {
        const float wt = __builtin_amdgcn_exp2f((mlw[w][0] - M) * LOG2E);
        L = fmaf(wt, mlw[w][1], L); o += wt * av[w];
      }
      __builtin_amdgcn_raw_buffer_store_b128(GRAN2(o[0], o[1]), rws, po, 0, AUX_SC1);
      __builtin_amdgcn_raw_buffer_store_b128(GRAN2(o[2], o[3]), rws, po + 1024, 0, AUX_SC1);
      __builtin_amdgcn_raw_buffer_store_b128(GRAN2(M, L), rws, mo, 0, AUX_SC1);
      flag[3] = 1u;
      ST(5);
    }
    STFLUSH(); PTFLUSH();
  } else {
    __builtin_amdgcn_s_setprio(3);
    const int hw = wave;
    const rsrc_t rwq = mkrs(wq, D * NQ * 2), rx = mkrs(x, 2 * D * 2);
    const int cb = s & 3, rq = (s >> 2) + 2 * b;
    const int qrow0 = 144 * rq + 36 * hw + (lane >> 3);
    const unsigned epoch = __builtin_amdgcn_raw_buffer_load_b32(rws, WS_EPOCH, 0, AUX_SC1) + 1u;
    SB();
    u32x3 xr[3];
#pragma unroll
    for (int i = 0; i < 3; ++i) xr[i] = __builtin_amdgcn_raw_buffer_load_b96(rx, lane * 36 + 12 * i, 0, 0);
    u32x4 wqr[5];
#pragma unroll
    for (int i = 0; i < 5; ++i) wqr[i] = __builtin_amdgcn_raw_buffer_load_b128(rwq, (i == 4 && lane >= 32) ? OOB : (qrow0 + 8 * i) * (NQ * 2) + (kvh * 256 + cb * 64 + (lane & 7) * 8) * 2, 0, AUX_NT);
    if (tid < 16) flag[lane] = lane == 8 ? 2u * (2 + NEXTRA) : 0u;
    LGKM0();
    SB();
    __builtin_amdgcn_s_barrier();
    SB();
    ST(1);
    const int gq = lane >> 4, c = lane & 15;
    {
      float v[18];
#pragma unroll
      for (int i = 0; i < 3; ++i)
#pragma unroll
        for (int j = 0; j < 3; ++j) { v[6 * i + 2 * j] = bflo(xr[i][j]); v[6 * i + 2 * j + 1] = bfhi(xr[i][j]); }
      float sm = 0.f;
#pragma unroll
      for (int i = 0; i < 18; ++i) sm += v[i];
      sm += dpp<XOR1>(sm); sm += dpp<XOR2>(sm); sm += dpp<ROR4>(sm); sm += dpp<ROR8>(sm); sm = x16_sum(sm);
      const float mean = sm * (1.f / D);
      float ss = 0.f;
#pragma unroll
      for (int i = 0; i < 18; ++i) { const float d = v[i] - mean; ss = fmaf(d, d, ss); }
      ss += dpp<XOR1>(ss); ss += dpp<XOR2>(ss); ss += dpp<ROR4>(ss); ss += dpp<ROR8>(ss); ss = x16_sum(ss);
      const float mb = rbf(mean), den = rbf(sqrtf(rbf(rbf(ss * (1.f / D)) + 1e-5f))), rden = 1.f / den;
#pragma unroll
      for (int i = 0; i < 18; i += 2) {
        float2 o; o.x = rbf(fdiv(rbf(v[i] - mb), den, rden)); o.y = rbf(fdiv(rbf(v[i + 1] - mb), den, rden));
        *reinterpret_cast<float2*>(lds + L_XN + lane * 18 + i) = o;
      }
      LGKM0();
      float a0[8], a1[8];
#pragma unroll
      for (int j = 0; j < 8; ++j) { a0[j] = 0.f; a1[j] = 0.f; }
#pragma unroll
      for (int i = 0; i < 5; ++i) {
        const int rr = i == 4 ? min(qrow0 + 32, D - 1) : qrow0 + 8 * i;
        const float x0 = lds[L_XN + rr], x1 = lds[L_XN + D + rr];
#pragma unroll
        for (int j = 0; j < 4; ++j) {
          const float lo = bflo(wqr[i][j]), hi = bfhi(wqr[i][j]);
          a0[2 * j] = fmaf(x0, lo, a0[2 * j]); a0[2 * j + 1] = fmaf(x0, hi, a0[2 * j + 1]);
          a1[2 * j] = fmaf(x1, lo, a1[2 * j]); a1[2 * j + 1] = fmaf(x1, hi, a1[2 * j + 1]);
        }
      }
      float r1[8], r2[4];
#pragma unroll
      for (int j = 0; j < 8; ++j) r1[j] = t32_sum(a0[j], a1[j]);
#pragma unroll
      for (int j = 0; j < 4; ++j) { r2[j] = t16_sum(r1[j], r1[4 + j]); r2[j] += dpp<ROR8>(r2[j]); }
      if ((lane & 8) == 0) *reinterpret_cast<f32x4*>(lds + L_QRED + hw * 128 + (lane >> 5) * 64 + (lane & 7) * 8 + ((lane >> 4) & 1) * 4) = f32x4{r2[0], r2[1], r2[2], r2[3]};
      CFENCE();
      (void)LDSADD(6, 1u);
    }
    ST(2);
    if (hw) {
      const rsrc_t rwg = mkrs(wg, D * F * 2), rwu = mkrs(wu, D * F * 2), rwd = mkrs(wd, F * D * 2), rwo = mkrs(wo, NQ * D * 2);
      const int colA = (hw - 1) * 192 + 8 * c, colB = (hw - 1) * 192 + 128 + 4 * c;
      const int frow0 = 192 * (hw - 1) + (lane >> 2); constexpr int FST = 16;
      u32x4 gw[12], uw[12], wa[8], fr[12]; u32x2 wb[8];
#define LDW(rs, row0) _Pragma("unroll") for (int j = 0; j < 8; ++j) { \
      VMW(FFN_T - 1); wa[j] = __builtin_amdgcn_raw_buffer_load_b128(rs, (8 * gq + j) * (D * 2) + colA * 2, (row0) * (D * 2), AUX_NT); SB(); \
      VMW(FFN_T - 1); wb[j] = __builtin_amdgcn_raw_buffer_load_b64(rs, (8 * gq + j) * (D * 2) + colB * 2, (row0) * (D * 2), AUX_NT); SB(); } \
      _Pragma("unroll") for (int cc = 0; cc < 4; ++cc) { \
        fr[2 * cc] = u32x4{perm_lo(wa[1][cc], wa[0][cc]), perm_lo(wa[3][cc], wa[2][cc]), perm_lo(wa[5][cc], wa[4][cc]), perm_lo(wa[7][cc], wa[6][cc])}; \
        fr[2 * cc + 1] = u32x4{perm_hi(wa[1][cc], wa[0][cc]), perm_hi(wa[3][cc], wa[2][cc]), perm_hi(wa[5][cc], wa[4][cc]), perm_hi(wa[7][cc], wa[6][cc])}; } \
      _Pragma("unroll") for (int cc = 0; cc < 2; ++cc) { \
        fr[8 + 2 * cc] = u32x4{perm_lo(wb[1][cc], wb[0][cc]), perm_lo(wb[3][cc], wb[2][cc]), perm_lo(wb[5][cc], wb[4][cc]), perm_lo(wb[7][cc], wb[6][cc])}; \
        fr[9 + 2 * cc] = u32x4{perm_hi(wb[1][cc], wb[0][cc]), perm_hi(wb[3][cc], wb[2][cc]), perm_hi(wb[5][cc], wb[4][cc]), perm_hi(wb[7][cc], wb[6][cc])}; }
#define YMFMA(AF) do { \
      f32x4 ya[12]; const f32x4 z4 = {0.f, 0.f, 0.f, 0.f}; \
      _Pragma("unroll") for (int j = 0; j < 12; ++j) ya[j] = mfma(AF, fr[j], z4); \
      const float a0_ = (gq & 1) ? ya[2][0] : ya[0][0], a1_ = (gq & 1) ? ya[6][0] : ya[4][0]; e0 += (gq & 2) ? a1_ : a0_; \
      const float b0_ = (gq & 1) ? ya[3][0] : ya[1][0], b1_ = (gq & 1) ? ya[7][0] : ya[5][0]; e1 += (gq & 2) ? b1_ : b0_; \
      e2 += (gq & 1) ? ya[10][0] : ya[8][0]; e3 += (gq & 1) ? ya[11][0] : ya[9][0]; } while (0)
      float e0 = 0.f, e1 = 0.f, e2 = 0.f, e3 = 0.f;
      const int nunit = s == s2 ? 2 : 1;
#pragma unroll 1
      for (int it = 0; it < nunit; ++it) {
        const int jl = s * 8 + (kvh >> 1), jr = jl % 7;
        const int line = it ? s * 8 + 3 : (jl == 63 ? 67 : (jl / 7) * 8 + jr + (jr >= 3));
        const int u0 = line * 64 + (kvh & 1) * 32;

#pragma unroll
        for (int i = 0; i < 12; ++i) {
          VMW(FFN_T - 1); gw[i] = __builtin_amdgcn_raw_buffer_load_b128(rwg, frow0 * (F * 2) + (lane & 3) * 16, FST * i * (F * 2) + u0 * 2, GAUX); SB();
          VMW(FFN_T - 1); uw[i] = __builtin_amdgcn_raw_buffer_load_b128(rwu, frow0 * (F * 2) + (lane & 3) * 16, FST * i * (F * 2) + u0 * 2, GAUX); SB();
        }
        float ag[8], au[8];
#pragma unroll
        for (int j = 0; j < 8; ++j) { ag[j] = 0.f; au[j] = 0.f; }
#pragma unroll
        for (int i = 0; i < 12; ++i) {
          const float xv = lds[L_XN + b * D + frow0 + FST * i];
#pragma unroll
          for (int j = 0; j < 4; ++j) {
            ag[2 * j] = fmaf(xv, bflo(gw[i][j]), ag[2 * j]); ag[2 * j + 1] = fmaf(xv, bfhi(gw[i][j]), ag[2 * j + 1]);
            au[2 * j] = fmaf(xv, bflo(uw[i][j]), au[2 * j]); au[2 * j + 1] = fmaf(xv, bfhi(uw[i][j]), au[2 * j + 1]);
          }
        }
        float r1[8], r2[4];
#pragma unroll
        for (int j = 0; j < 8; ++j) r1[j] = t32_sum(ag[j], au[j]);
#pragma unroll
        for (int j = 0; j < 4; ++j) { r2[j] = t16_sum(r1[j], r1[4 + j]); r2[j] += dpp<ROR4>(r2[j]); r2[j] += dpp<ROR8>(r2[j]); }
        if ((lane & 12) == 0) *reinterpret_cast<f32x4*>(lds + L_GU + (it * 3 + hw - 1) * 64 + (lane >> 5) * 32 + (lane & 3) * 8 + ((lane >> 4) & 1) * 4) = f32x4{r2[0], r2[1], r2[2], r2[3]};
        CFENCE();
        (void)LDSADD(4 + it, 1u);
        LDW(rwd, u0)
        for (unsigned spin = 0; flag[4 + it] < 3u && spin < (SPIN_CAP << 4); ++spin) __builtin_amdgcn_s_sleep(1);
        CFENCE();
        const int cc = lane & 31;
        const float* gu = lds + L_GU + it * 192 + cc;
        const float gt = rbf((gu[0] + gu[64]) + gu[128]), up = rbf((gu[32] + gu[96]) + gu[160]);
        const float y2 = -2.f * 0.7978845608028654f * LOG2E * (gt + 0.044715f * gt * gt * gt);
        const float ge = rbf(gt / (1.f + __builtin_amdgcn_exp2f(y2)));
        reinterpret_cast<__bf16*>(lds + L_H + hw * 16)[cc] = (__bf16)(ge * up);
        LGKM0();
        const u32x4 HA = *reinterpret_cast<const u32x4*>(lds + L_H + hw * 16 + gq * 4);
        YMFMA(HA);
        SB();
      }
      ST(4);
      LDW(rwo, kvh * 256 + s * 32)
      const int sp = (lane >> 1) & 7;
      int po = WS_PART + (g * 8 + sp) * 2048 + (s * 8 + gq * 2 + (lane & 1)) * 16, mo = WS_ML + g * 1024 + s * 128 + sp * 16;
      int yoA = WS_Y + ((b * 72 + (hw - 1) * 24 + (lane >> 2)) * 128 + kvh * 8 + s) * 64 + (lane & 3) * 16, yoB = lane < 32 ? yoA + 16 * 8192 : OOB;
#pragma unroll
      for (int j = 0; j < 12; ++j) asm volatile("" : "+v"(fr[j]));
      asm volatile("" : "+v"(e0), "+v"(e1), "+v"(e2), "+v"(e3), "+v"(po), "+v"(mo), "+v"(yoA), "+v"(yoB));
      ST(3);
      for (unsigned spin = 0; flag[3] == 0u && spin < (SPIN_CAP << 4); ++spin) __builtin_amdgcn_s_sleep(2);
      ST(5);
      u32x4 p0, p1, ml;
      for (unsigned spin = 0;; ++spin) {
        CFENCE();
        p0 = __builtin_amdgcn_raw_buffer_load_b128(rws, po, 0, AUX_SC1);
        p1 = __builtin_amdgcn_raw_buffer_load_b128(rws, po + 1024, 0, AUX_SC1);
        ml = __builtin_amdgcn_raw_buffer_load_b128(rws, mo, 0, AUX_SC1);
        const bool ok = (((p0[1] ^ epoch) | (p0[3] ^ epoch) | (p1[1] ^ epoch)) | ((p1[3] ^ epoch) | (ml[1] ^ epoch) | (ml[3] ^ epoch))) == 0u;
        if (__all(ok) || spin > SPIN_CAP) break;
      }
      ST(6);
      const float ms = __uint_as_float(ml[0]);
      float M = ms; M = fmaxf(M, dpp<XOR2>(M)); M = fmaxf(M, dpp<ROR4>(M)); M = fmaxf(M, dpp<ROR8>(M));
      const float wt = __builtin_amdgcn_exp2f((ms - M) * LOG2E);
      float v[5] = {wt * __uint_as_float(p0[0]), wt * __uint_as_float(p0[2]), wt * __uint_as_float(p1[0]), wt * __uint_as_float(p1[2]), wt * __uint_as_float(ml[2])};
#pragma unroll
      for (int j = 0; j < 5; ++j) { v[j] += dpp<XOR2>(v[j]); v[j] += dpp<ROR4>(v[j]); v[j] += dpp<ROR8>(v[j]); }
      const float r0 = __builtin_amdgcn_rcpf(v[4]), inv = fmaf(fmaf(-v[4], r0, 1.f), r0, r0);
      const unsigned o0 = pkbf(v[0] * inv, v[1] * inv), o1 = pkbf(v[2] * inv, v[3] * inv);
      const u32x4 OA = {dppu<0xA0>(o0), dppu<0xA0>(o1), dppu<0xF5>(o0), dppu<0xF5>(o1)};
      YMFMA(OA);
      __builtin_amdgcn_raw_buffer_store_b128(GRAN2(e0, e1), rws, yoA, 0, AUX_SC1);
      __builtin_amdgcn_raw_buffer_store_b128(GRAN2(e2, e3), rws, yoB, 0, AUX_SC1);
      flag[7] = 1u;
      ST(7);
    } else {
      for (unsigned spin = 0; flag[6] < 4u && spin < (SPIN_CAP << 4); ++spin) {}
      CFENCE();
      {
        const float2 q0 = *reinterpret_cast<const float2*>(lds + L_QRED + 2 * lane), q1 = *reinterpret_cast<const float2*>(lds + L_QRED + 128 + 2 * lane);
        const float2 q2 = *reinterpret_cast<const float2*>(lds + L_QRED + 256 + 2 * lane), q3 = *reinterpret_cast<const float2*>(lds + L_QRED + 384 + 2 * lane);
        const float qa = (q0.x + q1.x) + (q2.x + q3.x), qb = (q0.y + q1.y) + (q2.y + q3.y);
        __builtin_amdgcn_raw_buffer_store_b128(GRAN2(qa, qb), rws, WS_QP + (((kvh * 2 + (lane >> 5)) * 4 + rq) * 256 + cb * 64 + 2 * (lane & 31)) * 8, 0, AUX_SC1);
      }
      ST(3);
#define POLL8(P, base, SLP) do { for (unsigned spin = 0;; ++spin) { CFENCE(); if (SLP) __builtin_amdgcn_s_sleep(SLP); \
        _Pragma("unroll") for (int r_ = 0; r_ < 8; ++r_) P[r_] = __builtin_amdgcn_raw_buffer_load_b128(rws, lane * 16 + (r_ & 3) * 1024, (base) + (r_ >> 2) * 4096, AUX_SC1); \
        unsigned bad_ = 0; _Pragma("unroll") for (int r_ = 0; r_ < 8; ++r_) bad_ |= (P[r_][1] ^ epoch) | (P[r_][3] ^ epoch); \
        if (__all(bad_ == 0u) || spin > SPIN_CAP) break; } } while (0)
      {
        u32x4 p[8];
        if (QSLEEP0) __builtin_amdgcn_s_sleep(QSLEEP0);
        POLL8(p, WS_QP + (kvh * 2 + b) * 8192, QSLEEP);
#pragma unroll
        for (int hh = 0; hh < 2; ++hh) {
          const float qa = (__uint_as_float(p[hh][0]) + __uint_as_float(p[2 + hh][0])) + (__uint_as_float(p[4 + hh][0]) + __uint_as_float(p[6 + hh][0]));
          const float qb = (__uint_as_float(p[hh][2]) + __uint_as_float(p[2 + hh][2])) + (__uint_as_float(p[4 + hh][2]) + __uint_as_float(p[6 + hh][2]));
          reinterpret_cast<unsigned*>(lds + L_Q)[hh * 64 + lane] = pkbf(qa, qb);
        }
      }
      flag[0] = epoch;
      CFENCE();
      flag[1] = 1u;
      ST(4);
      if (POOL) {
        if (s == 0) {
          __builtin_amdgcn_raw_buffer_store_b32(0u, rws, lane == 0 ? WS_CNT + g * 256 : OOB, 0, AUX_SC1);
          asm volatile("s_waitcnt vmcnt(0)" ::: "memory");
          __builtin_amdgcn_raw_buffer_store_b32(epoch, rws, lane == 0 ? WS_CNT + g * 256 + 128 : OOB, 0, AUX_SC1);
        }
        for (unsigned spin = 0; (int)flag[8] < LBASE - 24 && spin < (SPIN_CAP << 2); ++spin) __builtin_amdgcn_s_sleep(32);
        for (unsigned spin = 0;; ++spin) {
          CFENCE();
          const unsigned f = __builtin_amdgcn_raw_buffer_load_b32(rws, WS_CNT + g * 256 + 128, 0, AUX_SC1);
          if (__all(f == epoch) || spin > SPIN_CAP) break;
          __builtin_amdgcn_s_sleep(8);
        }
        const rsrc_t rc = mkrs(ws + WS_CNT + g * 256, 4);
        int req = 0, left = 0;
        for (unsigned spin = 0; spin < (SPIN_CAP << 4) && req < LMAX; ++spin) {
          int nn = (int)__builtin_amdgcn_readfirstlane((int)flag[8]) - LBASE + LEAD - req;
          if (nn <= 0) { __builtin_amdgcn_s_sleep(2); continue; }
          nn = min(max(nn, left / GSS), 24);
          const unsigned t0 = (unsigned)__builtin_amdgcn_readfirstlane(__builtin_amdgcn_raw_ptr_buffer_atomic_add_i32(nn, rc, lane == 0 ? 0 : 64, 0, 0));
          left = max(NTK - (int)t0 - GSSA * nn, 0);
          const int got = t0 >= (unsigned)NTK ? 0 : min(nn, NTK - (int)t0);
          const unsigned tt = t0 + (unsigned)lane;
          if (lane < got) list[req + lane] = (tt & 7) * 128 + 2 * NP0 + (tt >> 3);
          req += got;
          if (got < nn || (int)t0 + nn >= NTK) break;
        }
        if (lane >= req) list[lane] = 0x7fffffffu;
        if (lane + 64 >= req) list[lane + 64] = 0x7fffffffu;
      }
      if (blockIdx.x < 144) {
        const int rid = blockIdx.x, rb = rid % 72, rw = rb / 24, rr = rb % 24;
        const int cc = (rr & 3) * 4 + (lane & 3);
        const int od = (rid / 72) * (D / 2) + rw * 96 + (rr < 16 ? 4 * cc + (rr >> 2) : 64 + 2 * cc + ((rr >> 2) & 1));
        const unsigned xres = __builtin_amdgcn_raw_buffer_load_b32(rx, od * 4, 0, 0);
        for (unsigned spin = 0; flag[7] == 0u && spin < (SPIN_CAP << 4); ++spin) __builtin_amdgcn_s_sleep(2);
        ST(5);
        u32x4 y[8];
        POLL8(y, WS_Y + rid * 8192, 0);
        ST(6);
        float s0 = 0.f, s1 = 0.f;
#pragma unroll
        for (int i = 0; i < 8; ++i) { s0 += __uint_as_float(y[i][0]); s1 += __uint_as_float(y[i][2]); }
        float st = t32_sum(s0, s1);
        st = x16_sum(st); st += dpp<ROR4>(st); st += dpp<ROR8>(st);
        const auto sw = __builtin_amdgcn_permlane32_swap(__float_as_uint(st), __float_as_uint(st), false, false);
        if (lane < 4) reinterpret_cast<unsigned*>(out)[od] = pkbf(bflo(xres) + __uint_as_float(sw[0]), bfhi(xres) + __uint_as_float(sw[1]));
        if (rid == 0 && lane == 0) __builtin_amdgcn_raw_buffer_store_b32(epoch, rws, WS_EPOCH, 0, AUX_SC1);
        ST(7);
      }
    }
    STFLUSH();
  }
}
}

extern "C" void kernel_launch(void* const* d_in, const int* in_sizes, int n_in, void* d_out, int out_size, void* d_ws, size_t ws_size, hipStream_t stream) {
  using fk::u16;
  fk::fused<<<256, 512, 0, stream>>>((const u16*)d_in[0], (const u16*)d_in[1], (const u16*)d_in[2], (const u16*)d_in[3], (const u16*)d_in[4],
                                     (const u16*)d_in[6], (const u16*)d_in[7], (const u16*)d_in[8], (u16*)d_out, (unsigned char*)d_ws);
}
```

```cpp
#include <hip/hip_runtime.h>
#include <hip/hip_bf16.h>
#include <stdint.h>

namespace fk {
typedef unsigned short u16;
typedef unsigned u32x4 __attribute__((ext_vector_type(4)));
typedef unsigned u32x2 __attribute__((ext_vector_type(2)));
typedef unsigned u32x3 __attribute__((ext_vector_type(3)));
typedef float f32x4 __attribute__((ext_vector_type(4)));
typedef float f32x2 __attribute__((ext_vector_type(2)));
typedef __bf16 bf16x8 __attribute__((ext_vector_type(8)));
typedef __bf16 bf16x2 __attribute__((ext_vector_type(2)));
#define LAS __attribute__((address_space(3)))
#define SB() __builtin_amdgcn_sched_barrier(0)

constexpr int D = 576, SK = 32768, DH = 128, F = 4608, NQ = 4096;
constexpr int POOL = 8;
constexpr int CLAIM = 4;
constexpr int XK = 2;
constexpr int NEXTRA = 4;
constexpr int PRE_T = 12;
constexpr int FFN_T = 3;
constexpr int CODE_B = 18 * 1024;
constexpr int PACE1 = 950, PACE2 = 890, PACETB = 56, PACEOFF = 175;
constexpr int CHUNK_B = 8192, PAIR_B = 16384;
constexpr int NP0 = (128 - POOL) / 2;
constexpr int NTK = 8 * POOL;
constexpr int GROUP_B = SK * DH * 2;
constexpr int NROW = 72;
static_assert(POOL % 2 == 0 && NTK <= 96 && NEXTRA % 2 == 0 && NP0 + XK < NROW && NP0 - 7 * XK >= 2 * (2 + NEXTRA), "");
constexpr int WS_EPOCH = 0, WS_CNT = 4096, WS_QP = 16384, WS_PART = WS_QP + 16 * 2 * 4 * 256 * 8, WS_ML = WS_PART + 32 * 8 * 256 * 8,
              WS_Y = WS_ML + 32 * 8 * 8 * 16, WS_END = WS_Y + 144 * 128 * 64;
constexpr int AUX_NT = 2, AUX_SC1 = 16, RSRC3 = 0x00020000, OOB = 0x7f000000;
constexpr unsigned SPIN_CAP = 1u << 18;
constexpr int L_XN = 0;
constexpr int L_QRED = L_XN + 2 * D;
constexpr int L_Q = L_QRED + 4 * 128;
constexpr int L_GU = L_Q + 128;
constexpr int L_H = L_GU + 2 * 3 * 64;
constexpr int L_ACC = L_H + 64;
constexpr int L_ML = L_ACC + 4 * 256;
constexpr int L_FLAG = L_ML + 16;
constexpr int L_DUMMY = L_FLAG + 16;
constexpr int L_LIST = L_DUMMY + 64;
constexpr int L_SC = L_LIST + 128;
constexpr int L_WORDS = L_SC + NROW * 64;
#define VMW(n) __builtin_amdgcn_s_waitcnt(0x0F70 | ((n) & 15) | (((n) >> 4) << 14))

__device__ __forceinline__ float bflo(unsigned v) { return __uint_as_float(v << 16); }
__device__ __forceinline__ float bfhi(unsigned v) { return __uint_as_float(v & 0xffff0000u); }
__device__ __forceinline__ unsigned pkbf(float lo, float hi) { bf16x2 r = {(__bf16)lo, (__bf16)hi}; return __builtin_bit_cast(unsigned, r); }
__device__ __forceinline__ float rbf(float f) { return (float)(__bf16)f; }
__device__ __forceinline__ float fdiv(float a, float d, float r) { const float q = a * r; return fmaf(fmaf(-q, d, a), r, q); }
template <int CTRL> __device__ __forceinline__ float dpp(float x) {
  return __builtin_bit_cast(float, __builtin_amdgcn_mov_dpp(__builtin_bit_cast(int, x), CTRL, 0xf, 0xf, true));
}
template <int CTRL> __device__ __forceinline__ unsigned dppu(unsigned x) { return (unsigned)__builtin_amdgcn_mov_dpp((int)x, CTRL, 0xf, 0xf, true); }
template <int CTRL, int BANK> __device__ __forceinline__ float dpp_bank(float old, float x) {
  return __builtin_bit_cast(float, __builtin_amdgcn_update_dpp(__builtin_bit_cast(int, old), __builtin_bit_cast(int, x), CTRL, 0xf, BANK, false));
}
constexpr int XOR1 = 0xB1, XOR2 = 0x4E, ROR4 = 0x124, ROR8 = 0x128, ROR12 = 0x12C, IDENT = 0xE4;
__device__ __forceinline__ float x16_max(float x) { auto s = __builtin_amdgcn_permlane16_swap(__float_as_uint(x), __float_as_uint(x), false, false); return fmaxf(__uint_as_float(s[0]), __uint_as_float(s[1])); }
__device__ __forceinline__ float x32_max(float x) { auto s = __builtin_amdgcn_permlane32_swap(__float_as_uint(x), __float_as_uint(x), false, false); return fmaxf(__uint_as_float(s[0]), __uint_as_float(s[1])); }
__device__ __forceinline__ float x16_sum(float x) { auto s = __builtin_amdgcn_permlane16_swap(__float_as_uint(x), __float_as_uint(x), false, false); return __uint_as_float(s[0]) + __uint_as_float(s[1]); }
__device__ __forceinline__ float x32_sum(float x) { auto s = __builtin_amdgcn_permlane32_swap(__float_as_uint(x), __float_as_uint(x), false, false); return __uint_as_float(s[0]) + __uint_as_float(s[1]); }
__device__ __forceinline__ float t32_sum(float a, float b) { auto s = __builtin_amdgcn_permlane32_swap(__float_as_uint(a), __float_as_uint(b), false, false); return __uint_as_float(s[0]) + __uint_as_float(s[1]); }
__device__ __forceinline__ float t16_sum(float a, float b) { auto s = __builtin_amdgcn_permlane16_swap(__float_as_uint(a), __float_as_uint(b), false, false); return __uint_as_float(s[0]) + __uint_as_float(s[1]); }
__device__ __forceinline__ f32x4 mfma(u32x4 a, u32x4 b, f32x4 c) {
  return __builtin_amdgcn_mfma_f32_16x16x32_bf16(__builtin_bit_cast(bf16x8, a), __builtin_bit_cast(bf16x8, b), c, 0, 0, 0);
}
__device__ __forceinline__ unsigned perm_lo(unsigned odd, unsigned even) { return __builtin_amdgcn_perm(odd, even, 0x05040100u); }
__device__ __forceinline__ unsigned perm_hi(unsigned odd, unsigned even) { return __builtin_amdgcn_perm(odd, even, 0x07060302u); }
typedef __amdgpu_buffer_rsrc_t rsrc_t;
__device__ __forceinline__ rsrc_t mkrs(const void* p, int bytes) { return __builtin_amdgcn_make_buffer_rsrc((void*)p, 0, bytes, RSRC3); }
#define LDSADD(idx, inc) __hip_atomic_fetch_add((LAS unsigned*)(LAS float*)(lds + (lane == 0 ? L_FLAG + (idx) : L_DUMMY + lane)), inc, __ATOMIC_RELAXED, __HIP_MEMORY_SCOPE_WORKGROUP)
#define LGKM0() asm volatile("s_waitcnt lgkmcnt(0)" ::: "memory")
#define CFENCE() asm volatile("" ::: "memory")
#define GRAN2(a, b) u32x4{__float_as_uint(a), epoch, __float_as_uint(b), epoch}
#define LDSWAIT(COND, SLP) for (unsigned spin = 0; (COND) && spin < (SPIN_CAP << 2); ++spin) __builtin_amdgcn_s_sleep(SLP)

__global__ void __launch_bounds__(512, 2) fused(const u16* __restrict__ x, const u16* __restrict__ kc, const u16* __restrict__ vc, const u16* __restrict__ wq,
                                                const u16* __restrict__ wo, const u16* __restrict__ wg, const u16* __restrict__ wu, const u16* __restrict__ wd,
                                                u16* out, unsigned char* ws) {
  __shared__ __attribute__((aligned(16))) float lds[L_WORDS];
  volatile LAS unsigned* flag = (volatile LAS unsigned*)(LAS float*)(lds + L_FLAG);
  volatile LAS unsigned* list = (volatile LAS unsigned*)(LAS float*)(lds + L_LIST);
  const int tid = threadIdx.x, lane = tid & 63, wave8 = __builtin_amdgcn_readfirstlane(tid >> 6), wave = wave8 & 3;
  const int g = blockIdx.x >> 3, s = blockIdx.x & 7, b = g >> 4, kvh = g & 15;
  const unsigned tstart = (unsigned)__builtin_amdgcn_s_memrealtime();
  const int s2 = kvh >> 1;
  const int NP = s == s2 ? NP0 - 7 * XK : NP0 + XK, LBASE = 2 * NP;
  constexpr float SCALE = 0.088388347648318440f, LOG2E = 1.4426950408889634f;
  unsigned long long pc0;
  asm volatile("s_getpc_b64 %0" : "=s"(pc0));
  const u32x4 cpf = __builtin_amdgcn_raw_buffer_load_b128(mkrs((const void*)(pc0 & ~255ull), CODE_B), lane * 16, wave8 == 4 ? g * 1024 : OOB, 0);
  SB();
  asm volatile("" :: "s"(x), "s"(kc), "s"(vc), "s"(wq), "s"(wo), "s"(wg), "s"(wu), "s"(wd), "s"(out), "s"(ws));
  const rsrc_t rws = mkrs(ws, WS_END);
  if (wave8 >= 4) {
    const u16* kb = kc + (size_t)g * (SK * DH); const u16* vb = vc + (size_t)g * (SK * DH);
    const int voffK = ((lane >> 2) & 3) * 256 + (lane >> 4) * 64 + (lane & 3) * 16, voffV = lane * 16;
    const rsrc_t rk = mkrs(kb, GROUP_B), rv = mkrs(vb, GROUP_B);
    const int obase = (s ^ (g >> 2)) << 20, xbase = ((s2 ^ (g >> 2)) << 20) + ((s - (s > s2)) * XK - 7 * XK) * PAIR_B;
    const int tsc = s == s2 ? (128 * 256) / (2 * (NP0 - 7 * XK) + POOL) : (128 * 256) / (2 * (NP0 + XK) + POOL);
#define POFF(p) (((p) < NP0 ? obase : xbase) + (p) * PAIR_B)
#define PACEWAIT(T) do { const int x_ = ((T) * tsc) >> 4; const unsigned due_ = (unsigned)(PACEOFF + ((min(x_, PACETB * 16) * PACE1 + max(x_ - PACETB * 16, 0) * PACE2) >> 8)); \
      for (unsigned spin = 0; (unsigned)__builtin_amdgcn_s_memrealtime() - tstart < due_ && spin < 4096u; ++spin) __builtin_amdgcn_s_sleep(2); } while (0)
    u32x4 R[2 + NEXTRA][8];
#define LD1(d, u, rs, vo, so) R[d][u] = __builtin_amdgcn_raw_buffer_load_b128(rs, (vo) + ((u) & 3) * 1024, (so) + ((u) >> 2) * 4096, AUX_NT)
#define PREFILL(LO, HI) _Pragma("unroll") for (int i = (LO); i < (HI); ++i) { if (i >= PRE_T) VMW(PRE_T - 1); \
      LD1(i >> 3, i & 7, rk, voffK, POFF((i >> 4) * 4 + wave) + ((i >> 3) & 1) * CHUNK_B); SB(); }
    PREFILL(0, 8)
    {
      const u32x4 sent = {~0u, ~0u, ~0u, ~0u}, ninf = {0xff80ff80u, 0xff80ff80u, 0xff80ff80u, 0xff80ff80u};
#pragma unroll
      for (int i = 0; i < 4; ++i) *reinterpret_cast<u32x4*>(lds + L_SC + i * 1024 + wave * 256 + lane * 4) = sent;
      if (wave < 2) *reinterpret_cast<u32x4*>(lds + L_SC + 4096 + wave * 256 + lane * 4) = (wave == 1 && lane >= 48) ? ninf : sent;
      else reinterpret_cast<unsigned*>(lds + L_LIST)[(wave - 2) * 64 + lane] = ~0u;
      LGKM0();
    }
    __builtin_amdgcn_s_barrier();
    SB();
    PREFILL(8, 8 * (2 + NEXTRA))
    asm volatile("" :: "v"(cpf));
    LDSWAIT(flag[1] == 0u, 1);
    CFENCE();
    const unsigned epoch = flag[0];
    const int n = lane & 15;
    const u32x4 qv = *reinterpret_cast<const u32x4*>(reinterpret_cast<const u16*>(lds + L_Q) + (n & 1) * 128 + (4 * (lane >> 4) + (n >> 2)) * 8);
    const u32x4 zero4 = {0u, 0u, 0u, 0u};
    const u32x4 Q0 = (n & 2) ? zero4 : qv, Q1 = (n & 2) ? qv : zero4;
    volatile LAS unsigned* scw = (volatile LAS unsigned*)(LAS float*)(lds + L_SC) + lane;
#define QK(d, rs, vo, so, SC, REFILL) do { \
      f32x4 dd[4]; \
      _Pragma("unroll") for (int p = 0; p < 4; ++p) { dd[p] = mfma(R[d][2 * p], Q0, f32x4{0.f, 0.f, 0.f, 0.f}); dd[p] = mfma(R[d][2 * p + 1], Q1, dd[p]); SB(); \
        if (REFILL) { LD1(d, 2 * p, rs, vo, so); SB(); LD1(d, 2 * p + 1, rs, vo, so); SB(); } } \
      float x0 = dpp<IDENT>(dd[0][0]), x1 = dpp<ROR12>(dd[0][1]), x2 = dpp<ROR8>(dd[0][2]), x3 = dpp<ROR4>(dd[0][3]); \
      x0 = dpp_bank<ROR4, 2>(x0, dd[1][0]); x1 = dpp_bank<IDENT, 2>(x1, dd[1][1]); x2 = dpp_bank<ROR12, 2>(x2, dd[1][2]); x3 = dpp_bank<ROR8, 2>(x3, dd[1][3]); \
      x0 = dpp_bank<ROR8, 4>(x0, dd[2][0]); x1 = dpp_bank<ROR4, 4>(x1, dd[2][1]); x2 = dpp_bank<IDENT, 4>(x2, dd[2][2]); x3 = dpp_bank<ROR12, 4>(x3, dd[2][3]); \
      x0 = dpp_bank<ROR12, 8>(x0, dd[3][0]); x1 = dpp_bank<ROR8, 8>(x1, dd[3][1]); x2 = dpp_bank<ROR4, 8>(x2, dd[3][2]); x3 = dpp_bank<IDENT, 8>(x3, dd[3][3]); \
      SC = rbf((x0 + x1) + (x2 + x3)) * SCALE; } while (0)
#define PV(d, rs, vo, so, SC) do { \
      const float pp = __builtin_amdgcn_exp2f(((SC) - m) * LOG2E); \
      l += pp; \
      const unsigned w_ = pkbf(pp, dpp<0x102>(pp)); \
      const u32x4 PA = {w_, dppu<0x104>(w_), dppu<0x108>(w_), dppu<0x10C>(w_)}; \
      u32x4 bf[8]; \
      SB(); \
      _Pragma("unroll") for (int t_ = 0; t_ < 4; ++t_) { \
        _Pragma("unroll") for (int c = 0; c < 4; ++c) { bf[2 * c][t_] = perm_lo(R[d][2 * t_ + 1][c], R[d][2 * t_][c]); bf[2 * c + 1][t_] = perm_hi(R[d][2 * t_ + 1][c], R[d][2 * t_][c]); } \
        SB(); LD1(d, 2 * t_, rs, vo, so); SB(); LD1(d, 2 * t_ + 1, rs, vo, so); SB(); } \
      _Pragma("unroll") for (int j = 0; j < 8; ++j) acc[j] = mfma(PA, bf[j], acc[j]); \
      SB(); } while (0)
#define RESCALE(SC) do { if (__any((SC) > m)) { \
        float pm = (SC); pm = fmaxf(pm, dpp<XOR2>(pm)); pm = fmaxf(pm, dpp<ROR4>(pm)); pm = fmaxf(pm, dpp<ROR8>(pm)); pm = x16_max(pm); pm = x32_max(pm); \
        const float mn = fmaxf(m, pm), al = __builtin_amdgcn_exp2f((m - mn) * LOG2E); \
        const float al0 = dpp<0x00>(al), al1 = dpp<0x55>(al); \
        l *= al; m = mn; \
        _Pragma("unroll") for (int j = 0; j < 8; ++j) { acc[j][0] *= al0; acc[j][1] *= al1; } } } while (0)
#define POOLITEM(SO) do { const int li = min(t - LBASE, 127); \
        unsigned e = (unsigned)__builtin_amdgcn_readfirstlane((int)list[li]); \
        for (unsigned spin = 0; e == ~0u && spin < (SPIN_CAP << 2); ++spin) e = (unsigned)__builtin_amdgcn_readfirstlane((int)list[li]); \
        SO = e < 1024u ? (int)e * CHUNK_B : GROUP_B; } while (0)
    float m = -3.0e38f;
    int cp = wave, t;
    unsigned lt = LDSADD(8, 1u);
    bool first = true;
    do {
      t = __builtin_amdgcn_readfirstlane((int)lt);
      lt = LDSADD(8, t < LBASE ? 1u : 0u);
      const bool nk = t < NP;
      const int np = nk ? t : (t < LBASE ? t - NP : NROW - 1);
      const rsrc_t rs = mkrs(nk ? kb : vb, GROUP_B);
      const int vo = nk ? voffK : voffV, so = np < NROW - 1 ? POFF(np) : GROUP_B;
      PACEWAIT(t);
      SB();
      { float s0, s1; QK(0, rs, vo, so, s0, true); QK(1, rs, vo, so + CHUNK_B, s1, true);
        const unsigned pk = min(pkbf(s0, s1), 0xfffffffeu); scw[cp * 64] = pk; m = fmaxf(fmaxf(m, bflo(pk)), bfhi(pk)); SB(); }
      if (first) {
#pragma unroll
        for (int d = 2; d < 2 + NEXTRA; d += 2) { float s0, s1; QK(d, rs, 0, 0, s0, false); QK(d + 1, rs, 0, 0, s1, false);
          const unsigned pk = min(pkbf(s0, s1), 0xfffffffeu); scw[((d >> 1) * 4 + wave) * 64] = pk; m = fmaxf(fmaxf(m, bflo(pk)), bfhi(pk)); SB(); }
        first = false;
      }
      cp = np;
    } while (t < NP);
    if (t >= LBASE) lt = (unsigned)t;
    m = fmaxf(m, dpp<XOR2>(m)); m = fmaxf(m, dpp<ROR4>(m)); m = fmaxf(m, dpp<ROR8>(m)); m = x16_max(m); m = x32_max(m);
    f32x4 acc[8];
#pragma unroll
    for (int j = 0; j < 8; ++j) acc[j] = f32x4{0.f, 0.f, 0.f, 0.f};
    float l = 0.f;
    int so;
    do {
      t = __builtin_amdgcn_readfirstlane((int)lt);
      lt = LDSADD(8, 1u);
      unsigned pk = scw[cp * 64];
      const bool nv = t < LBASE;
      so = POFF(t - NP);
      if (!nv) POOLITEM(so);
      const rsrc_t rs0 = mkrs(nv ? vb : kb, GROUP_B);
      const int vo0 = nv ? voffV : voffK, so1 = nv ? so + CHUNK_B : so;
      for (unsigned spin = 0; __any(pk == ~0u) && spin < (SPIN_CAP << 2); ++spin) pk = scw[cp * 64];
      const float sc0 = bflo(pk), sc1 = bfhi(pk), scm = fmaxf(sc0, sc1);
      RESCALE(scm);
      PACEWAIT(t);
      SB();
      PV(0, rs0, vo0, so, sc0);
      PV(1, rv, voffV, so1, sc1);
      cp = t - NP;
    } while (t < LBASE);
    while (so < GROUP_B) {
      t = __builtin_amdgcn_readfirstlane((int)lt);
      lt = LDSADD(8, 1u);
      POOLITEM(so);
      SB();
      float sc;
      QK(0, rk, voffK, so, sc, true);
      sc = rbf(sc);
      RESCALE(sc);
      PV(1, rv, voffV, so, sc);
    }
    l += dpp<XOR2>(l); l += dpp<ROR4>(l); l += dpp<ROR8>(l); l = x16_sum(l); l = x32_sum(l);
    {
      volatile LAS f32x4* aw = (volatile LAS f32x4*)(LAS float*)(lds + L_ACC + wave * 256 + lane * 8);
      if (lane < 16) {
#pragma unroll
        for (int h = 0; h < 2; ++h) {
          aw[h * 32] = f32x4{acc[0][h], acc[1][h], acc[2][h], acc[3][h]};
          aw[h * 32 + 1] = f32x4{acc[4][h], acc[5][h], acc[6][h], acc[7][h]};
        }
        if (lane < 2) *(volatile LAS f32x2*)(LAS float*)(lds + L_ML + (wave * 2 + lane) * 2) = f32x2{m, l};
      }
    }
    const unsigned old = LDSADD(2, 1u);
    CFENCE();
    f32x2 mlw[4]; f32x4 av[4];
#pragma unroll
    for (int w = 0; w < 4; ++w) {
      mlw[w] = *(volatile LAS f32x2*)(LAS float*)(lds + L_ML + w * 4 + (lane >> 5) * 2);
      av[w] = *(volatile LAS f32x4*)(LAS float*)(lds + L_ACC + w * 256 + lane * 4);
    }
    const int po = WS_PART + (g * 8 + s) * 2048 + lane * 16, mo = (lane & 7) == 0 ? WS_ML + g * 1024 + (lane >> 3) * 128 + s * 16 : OOB;
    if (__builtin_amdgcn_readfirstlane(old) == 3u) {
      const float M = fmaxf(fmaxf(mlw[0][0], mlw[1][0]), fmaxf(mlw[2][0], mlw[3][0]));
      float L = 0.f; f32x4 o = {0.f, 0.f, 0.f, 0.f};
#pragma unroll
      for (int w = 0; w < 4; ++w) {
        const float wt = __builtin_amdgcn_exp2f((mlw[w][0] - M) * LOG2E);
        L = fmaf(wt, mlw[w][1], L); o += wt * av[w];
      }
      __builtin_amdgcn_raw_buffer_store_b128(GRAN2(o[0], o[1]), rws, po, 0, AUX_SC1);
      __builtin_amdgcn_raw_buffer_store_b128(GRAN2(o[2], o[3]), rws, po + 1024, 0, AUX_SC1);
      __builtin_amdgcn_raw_buffer_store_b128(GRAN2(M, L), rws, mo, 0, AUX_SC1);
      flag[3] = 1u;
    }
  } else {
    __builtin_amdgcn_s_setprio(3);
    const int hw = wave;
    const rsrc_t rwq = mkrs(wq, D * NQ * 2), rx = mkrs(x, 2 * D * 2);
    const int cb = s & 3, rq = (s >> 2) + 2 * b;
    const int qrow0 = 144 * rq + 36 * hw + (lane >> 3);
    const unsigned epoch = __builtin_amdgcn_raw_buffer_load_b32(rws, WS_EPOCH, 0, AUX_SC1) + 1u;
    SB();
    u32x3 xr[3];
#pragma unroll
    for (int i = 0; i < 3; ++i) xr[i] = __builtin_amdgcn_raw_buffer_load_b96(rx, lane * 36 + 12 * i, 0, 0);
    u32x4 wqr[5];
#pragma unroll
    for (int i = 0; i < 5; ++i) wqr[i] = __builtin_amdgcn_raw_buffer_load_b128(rwq, (i == 4 && lane >= 32) ? OOB : (qrow0 + 8 * i) * (NQ * 2) + (kvh * 256 + cb * 64 + (lane & 7) * 8) * 2, 0, AUX_NT);
    if (tid < 16) flag[lane] = lane == 8 ? 2u * (2 + NEXTRA) : 0u;
    LGKM0();
    SB();
    __builtin_amdgcn_s_barrier();
    SB();
    const int gq = lane >> 4, c = lane & 15;
    {
      float v[18];
#pragma unroll
      for (int i = 0; i < 3; ++i)
#pragma unroll
        for (int j = 0; j < 3; ++j) { v[6 * i + 2 * j] = bflo(xr[i][j]); v[6 * i + 2 * j + 1] = bfhi(xr[i][j]); }
      float sm = 0.f;
#pragma unroll
      for (int i = 0; i < 18; ++i) sm += v[i];
      sm += dpp<XOR1>(sm); sm += dpp<XOR2>(sm); sm += dpp<ROR4>(sm); sm += dpp<ROR8>(sm); sm = x16_sum(sm);
      const float mean = sm * (1.f / D);
      float ss = 0.f;
#pragma unroll
      for (int i = 0; i < 18; ++i) { const float d = v[i] - mean; ss = fmaf(d, d, ss); }
      ss += dpp<XOR1>(ss); ss += dpp<XOR2>(ss); ss += dpp<ROR4>(ss); ss += dpp<ROR8>(ss); ss = x16_sum(ss);
      const float mb = rbf(mean), den = rbf(sqrtf(rbf(rbf(ss * (1.f / D)) + 1e-5f))), rden = 1.f / den;
#pragma unroll
      for (int i = 0; i < 18; i += 2) {
        float2 o; o.x = rbf(fdiv(rbf(v[i] - mb), den, rden)); o.y = rbf(fdiv(rbf(v[i + 1] - mb), den, rden));
        *reinterpret_cast<float2*>(lds + L_XN + lane * 18 + i) = o;
      }
      LGKM0();
      float a0[8], a1[8];
#pragma unroll
      for (int j = 0; j < 8; ++j) { a0[j] = 0.f; a1[j] = 0.f; }
#pragma unroll
      for (int i = 0; i < 5; ++i) {
        const int rr = i == 4 ? min(qrow0 + 32, D - 1) : qrow0 + 8 * i;
        const float x0 = lds[L_XN + rr], x1 = lds[L_XN + D + rr];
#pragma unroll
        for (int j = 0; j < 4; ++j) {
          const float lo = bflo(wqr[i][j]), hi = bfhi(wqr[i][j]);
          a0[2 * j] = fmaf(x0, lo, a0[2 * j]); a0[2 * j + 1] = fmaf(x0, hi, a0[2 * j + 1]);
          a1[2 * j] = fmaf(x1, lo, a1[2 * j]); a1[2 * j + 1] = fmaf(x1, hi, a1[2 * j + 1]);
        }
      }
      float r1[8], r2[4];
#pragma unroll
      for (int j = 0; j < 8; ++j) r1[j] = t32_sum(a0[j], a1[j]);
#pragma unroll
      for (int j = 0; j < 4; ++j) { r2[j] = t16_sum(r1[j], r1[4 + j]); r2[j] += dpp<ROR8>(r2[j]); }
      if ((lane & 8) == 0) *reinterpret_cast<f32x4*>(lds + L_QRED + hw * 128 + (lane >> 5) * 64 + (lane & 7) * 8 + ((lane >> 4) & 1) * 4) = f32x4{r2[0], r2[1], r2[2], r2[3]};
      CFENCE();
      (void)LDSADD(6, 1u);
    }
    if (hw) {
      const rsrc_t rwg = mkrs(wg, D * F * 2), rwu = mkrs(wu, D * F * 2), rwd = mkrs(wd, F * D * 2), rwo = mkrs(wo, NQ * D * 2);
      const int colA = (hw - 1) * 192 + 8 * c, colB = (hw - 1) * 192 + 128 + 4 * c;
      const int frow0 = 192 * (hw - 1) + (lane >> 2);
      u32x4 gw[12], uw[12], wa[8], fr[12]; u32x2 wb[8];
#define LDW(rs, row0) _Pragma("unroll") for (int j = 0; j < 8; ++j) { \
      VMW(FFN_T - 1); wa[j] = __builtin_amdgcn_raw_buffer_load_b128(rs, (8 * gq + j) * (D * 2) + colA * 2, (row0) * (D * 2), AUX_NT); SB(); \
      VMW(FFN_T - 1); wb[j] = __builtin_amdgcn_raw_buffer_load_b64(rs, (8 * gq + j) * (D * 2) + colB * 2, (row0) * (D * 2), AUX_NT); SB(); } \
      _Pragma("unroll") for (int cc = 0; cc < 4; ++cc) { \
        fr[2 * cc] = u32x4{perm_lo(wa[1][cc], wa[0][cc]), perm_lo(wa[3][cc], wa[2][cc]), perm_lo(wa[5][cc], wa[4][cc]), perm_lo(wa[7][cc], wa[6][cc])}; \
        fr[2 * cc + 1] = u32x4{perm_hi(wa[1][cc], wa[0][cc]), perm_hi(wa[3][cc], wa[2][cc]), perm_hi(wa[5][cc], wa[4][cc]), perm_hi(wa[7][cc], wa[6][cc])}; } \
      _Pragma("unroll") for (int cc = 0; cc < 2; ++cc) { \
        fr[8 + 2 * cc] = u32x4{perm_lo(wb[1][cc], wb[0][cc]), perm_lo(wb[3][cc], wb[2][cc]), perm_lo(wb[5][cc], wb[4][cc]), perm_lo(wb[7][cc], wb[6][cc])}; \
        fr[9 + 2 * cc] = u32x4{perm_hi(wb[1][cc], wb[0][cc]), perm_hi(wb[3][cc], wb[2][cc]), perm_hi(wb[5][cc], wb[4][cc]), perm_hi(wb[7][cc], wb[6][cc])}; }
#define YMFMA(AF) do { \
      f32x4 ya[12]; const f32x4 z4 = {0.f, 0.f, 0.f, 0.f}; \
      _Pragma("unroll") for (int j = 0; j < 12; ++j) ya[j] = mfma(AF, fr[j], z4); \
      const float a0_ = (gq & 1) ? ya[2][0] : ya[0][0], a1_ = (gq & 1) ? ya[6][0] : ya[4][0]; e0 += (gq & 2) ? a1_ : a0_; \
      const float b0_ = (gq & 1) ? ya[3][0] : ya[1][0], b1_ = (gq & 1) ? ya[7][0] : ya[5][0]; e1 += (gq & 2) ? b1_ : b0_; \
      e2 += (gq & 1) ? ya[10][0] : ya[8][0]; e3 += (gq & 1) ? ya[11][0] : ya[9][0]; } while (0)
      float e0 = 0.f, e1 = 0.f, e2 = 0.f, e3 = 0.f;
      const int nunit = s == s2 ? 2 : 1;
#pragma unroll 1
      for (int it = 0; it < nunit; ++it) {
        const int jl = s * 8 + (kvh >> 1), jr = jl % 7;
        const int line = it ? s * 8 + 3 : (jl == 63 ? 67 : (jl / 7) * 8 + jr + (jr >= 3));
        const int u0 = line * 64 + (kvh & 1) * 32;
#pragma unroll
        for (int i = 0; i < 12; ++i) {
          VMW(FFN_T - 1); gw[i] = __builtin_amdgcn_raw_buffer_load_b128(rwg, frow0 * (F * 2) + (lane & 3) * 16, 16 * i * (F * 2) + u0 * 2, AUX_NT); SB();
          VMW(FFN_T - 1); uw[i] = __builtin_amdgcn_raw_buffer_load_b128(rwu, frow0 * (F * 2) + (lane & 3) * 16, 16 * i * (F * 2) + u0 * 2, AUX_NT); SB();
        }
        float ag[8], au[8];
#pragma unroll
        for (int j = 0; j < 8; ++j) { ag[j] = 0.f; au[j] = 0.f; }
#pragma unroll
        for (int i = 0; i < 12; ++i) {
          const float xv = lds[L_XN + b * D + frow0 + 16 * i];
#pragma unroll
          for (int j = 0; j < 4; ++j) {
            ag[2 * j] = fmaf(xv, bflo(gw[i][j]), ag[2 * j]); ag[2 * j + 1] = fmaf(xv, bfhi(gw[i][j]), ag[2 * j + 1]);
            au[2 * j] = fmaf(xv, bflo(uw[i][j]), au[2 * j]); au[2 * j + 1] = fmaf(xv, bfhi(uw[i][j]), au[2 * j + 1]);
          }
        }
        float r1[8], r2[4];
#pragma unroll
        for (int j = 0; j < 8; ++j) r1[j] = t32_sum(ag[j], au[j]);
#pragma unroll
        for (int j = 0; j < 4; ++j) { r2[j] = t16_sum(r1[j], r1[4 + j]); r2[j] += dpp<ROR4>(r2[j]); r2[j] += dpp<ROR8>(r2[j]); }
        if ((lane & 12) == 0) *reinterpret_cast<f32x4*>(lds + L_GU + (it * 3 + hw - 1) * 64 + (lane >> 5) * 32 + (lane & 3) * 8 + ((lane >> 4) & 1) * 4) = f32x4{r2[0], r2[1], r2[2], r2[3]};
        CFENCE();
        (void)LDSADD(4 + it, 1u);
        LDW(rwd, u0)
        LDSWAIT(flag[4 + it] < 3u, 1);
        CFENCE();
        const int cc = lane & 31;
        const float* gu = lds + L_GU + it * 192 + cc;
        const float gt = rbf((gu[0] + gu[64]) + gu[128]), up = rbf((gu[32] + gu[96]) + gu[160]);
        const float y2 = -2.f * 0.7978845608028654f * LOG2E * (gt + 0.044715f * gt * gt * gt);
        const float ge = rbf(gt / (1.f + __builtin_amdgcn_exp2f(y2)));
        reinterpret_cast<__bf16*>(lds + L_H + hw * 16)[cc] = (__bf16)(ge * up);
        LGKM0();
        const u32x4 HA = *reinterpret_cast<const u32x4*>(lds + L_H + hw * 16 + gq * 4);
        YMFMA(HA);
        SB();
      }
      LDW(rwo, kvh * 256 + s * 32)
      const int sp = (lane >> 1) & 7;
      int po = WS_PART + (g * 8 + sp) * 2048 + (s * 8 + gq * 2 + (lane & 1)) * 16, mo = WS_ML + g * 1024 + s * 128 + sp * 16;
      int yoA = WS_Y + ((b * 72 + (hw - 1) * 24 + (lane >> 2)) * 128 + kvh * 8 + s) * 64 + (lane & 3) * 16, yoB = lane < 32 ? yoA + 16 * 8192 : OOB;
#pragma unroll
      for (int j = 0; j < 12; ++j) asm volatile("" : "+v"(fr[j]));
      asm volatile("" : "+v"(e0), "+v"(e1), "+v"(e2), "+v"(e3), "+v"(po), "+v"(mo), "+v"(yoA), "+v"(yoB));
      LDSWAIT((int)flag[8] < LBASE - 6, 32);
      if (hw == 1) {
        const u32x4 w0 = __builtin_amdgcn_raw_buffer_load_b128(rws, po, 0, AUX_SC1), w1 = __builtin_amdgcn_raw_buffer_load_b128(rws, po + 1024, 0, AUX_SC1), w2 = __builtin_amdgcn_raw_buffer_load_b128(rws, mo, 0, AUX_SC1);
        const u32x4 w3 = __builtin_amdgcn_raw_buffer_load_b128(mkrs((const void*)(pc0 & ~255ull), CODE_B + 512), lane * 16, g * 1024, 0);
        asm volatile("" :: "v"(w0), "v"(w1), "v"(w2), "v"(w3));
      } else {
        u32x4 w[4];
#pragma unroll
        for (int r_ = 0; r_ < 4; ++r_) w[r_] = __builtin_amdgcn_raw_buffer_load_b128(rws, lane * 16 + r_ * 1024, blockIdx.x < 144 ? WS_Y + blockIdx.x * 8192 + (hw - 2) * 4096 : OOB, AUX_SC1);
        asm volatile("" :: "v"(w[0]), "v"(w[1]), "v"(w[2]), "v"(w[3]));
      }
      LDSWAIT(flag[3] == 0u, 2);
      u32x4 p0, p1, ml;
      for (unsigned spin = 0;; ++spin) {
        CFENCE();
        p0 = __builtin_amdgcn_raw_buffer_load_b128(rws, po, 0, AUX_SC1);
        p1 = __builtin_amdgcn_raw_buffer_load_b128(rws, po + 1024, 0, AUX_SC1);
        ml = __builtin_amdgcn_raw_buffer_load_b128(rws, mo, 0, AUX_SC1);
        const bool ok = (((p0[1] ^ epoch) | (p0[3] ^ epoch) | (p1[1] ^ epoch)) | ((p1[3] ^ epoch) | (ml[1] ^ epoch) | (ml[3] ^ epoch))) == 0u;
        if (__all(ok) || spin > SPIN_CAP) break;
      }
      const float ms = __uint_as_float(ml[0]);
      float M = ms; M = fmaxf(M, dpp<XOR2>(M)); M = fmaxf(M, dpp<ROR4>(M)); M = fmaxf(M, dpp<ROR8>(M));
      const float wt = __builtin_amdgcn_exp2f((ms - M) * LOG2E);
      float v[5] = {wt * __uint_as_float(p0[0]), wt * __uint_as_float(p0[2]), wt * __uint_as_float(p1[0]), wt * __uint_as_float(p1[2]), wt * __uint_as_float(ml[2])};
#pragma unroll
      for (int j = 0; j < 5; ++j) { v[j] += dpp<XOR2>(v[j]); v[j] += dpp<ROR4>(v[j]); v[j] += dpp<ROR8>(v[j]); }
      const float r0 = __builtin_amdgcn_rcpf(v[4]), inv = fmaf(fmaf(-v[4], r0, 1.f), r0, r0);
      const unsigned o0 = pkbf(v[0] * inv, v[1] * inv), o1 = pkbf(v[2] * inv, v[3] * inv);
      const u32x4 OA = {dppu<0xA0>(o0), dppu<0xA0>(o1), dppu<0xF5>(o0), dppu<0xF5>(o1)};
      YMFMA(OA);
      __builtin_amdgcn_raw_buffer_store_b128(GRAN2(e0, e1), rws, yoA, 0, AUX_SC1);
      __builtin_amdgcn_raw_buffer_store_b128(GRAN2(e2, e3), rws, yoB, 0, AUX_SC1);
      flag[7] = 1u;
    } else {
      for (unsigned spin = 0; flag[6] < 4u && spin < (SPIN_CAP << 2); ++spin) {}
      CFENCE();
      {
        const float2 q0 = *reinterpret_cast<const float2*>(lds + L_QRED + 2 * lane), q1 = *reinterpret_cast<const float2*>(lds + L_QRED + 128 + 2 * lane);
        const float2 q2 = *reinterpret_cast<const float2*>(lds + L_QRED + 256 + 2 * lane), q3 = *reinterpret_cast<const float2*>(lds + L_QRED + 384 + 2 * lane);
        const float qa = (q0.x + q1.x) + (q2.x + q3.x), qb = (q0.y + q1.y) + (q2.y + q3.y);
        __builtin_amdgcn_raw_buffer_store_b128(GRAN2(qa, qb), rws, WS_QP + (((kvh * 2 + (lane >> 5)) * 4 + rq) * 256 + cb * 64 + 2 * (lane & 31)) * 8, 0, AUX_SC1);
      }
#define POLL8(P, base, SLP) do { for (unsigned spin = 0;; ++spin) { CFENCE(); if (SLP) __builtin_amdgcn_s_sleep(SLP); \
        _Pragma("unroll") for (int r_ = 0; r_ < 8; ++r_) P[r_] = __builtin_amdgcn_raw_buffer_load_b128(rws, lane * 16 + (r_ & 3) * 1024, (base) + (r_ >> 2) * 4096, AUX_SC1); \
        unsigned bad_ = 0; _Pragma("unroll") for (int r_ = 0; r_ < 8; ++r_) bad_ |= (P[r_][1] ^ epoch) | (P[r_][3] ^ epoch); \
        if (__all(bad_ == 0u) || spin > SPIN_CAP) break; } } while (0)
      {
        u32x4 p[8];
        __builtin_amdgcn_s_sleep(24);
        POLL8(p, WS_QP + (kvh * 2 + b) * 8192, 8);
#pragma unroll
        for (int hh = 0; hh < 2; ++hh) {
          const float qa = (__uint_as_float(p[hh][0]) + __uint_as_float(p[2 + hh][0])) + (__uint_as_float(p[4 + hh][0]) + __uint_as_float(p[6 + hh][0]));
          const float qb = (__uint_as_float(p[hh][2]) + __uint_as_float(p[2 + hh][2])) + (__uint_as_float(p[4 + hh][2]) + __uint_as_float(p[6 + hh][2]));
          reinterpret_cast<unsigned*>(lds + L_Q)[hh * 64 + lane] = pkbf(qa, qb);
        }
      }
      flag[0] = epoch;
      CFENCE();
      flag[1] = 1u;
      if (s == 0) {
        __builtin_amdgcn_raw_buffer_store_b32(0u, rws, lane == 0 ? WS_CNT + g * 256 : OOB, 0, AUX_SC1);
        asm volatile("s_waitcnt vmcnt(0)" ::: "memory");
        __builtin_amdgcn_raw_buffer_store_b32(epoch, rws, lane == 0 ? WS_CNT + g * 256 + 128 : OOB, 0, AUX_SC1);
      }
      LDSWAIT((int)flag[8] < LBASE - 24, 32);
      for (unsigned spin = 0;; ++spin) {
        CFENCE();
        const unsigned f = __builtin_amdgcn_raw_buffer_load_b32(rws, WS_CNT + g * 256 + 128, 0, AUX_SC1);
        if (__all(f == epoch) || spin > SPIN_CAP) break;
        __builtin_amdgcn_s_sleep(8);
      }
      {
        const rsrc_t rc = mkrs(ws + WS_CNT + g * 256, 4);
        int req = 0;
        for (unsigned spin = 0; spin < (SPIN_CAP << 2); ++spin) {
          if ((int)__builtin_amdgcn_readfirstlane((int)flag[8]) - LBASE <= req) { __builtin_amdgcn_s_sleep(2); continue; }
          const unsigned t0 = (unsigned)__builtin_amdgcn_readfirstlane(__builtin_amdgcn_raw_ptr_buffer_atomic_add_i32(CLAIM, rc, lane == 0 ? 0 : 64, 0, 0));
          const int got = t0 >= (unsigned)NTK ? 0 : min(CLAIM, NTK - (int)t0);
          const unsigned tt = t0 + (unsigned)lane;
          if (lane < got) list[req + lane] = (tt & 7) * 128 + 2 * NP0 + (tt >> 3);
          req += got;
          if (got < CLAIM || (int)t0 + CLAIM >= NTK || req >= NTK) break;
        }
        if (lane >= req) list[lane] = 0x7fffffffu;
        if (lane + 64 >= req) list[lane + 64] = 0x7fffffffu;
      }
      if (blockIdx.x < 144) {
        const int rid = blockIdx.x, rb = rid % 72, rw = rb / 24, rr = rb % 24;
        const int cc = (rr & 3) * 4 + (lane & 3);
        const int od = (rid / 72) * (D / 2) + rw * 96 + (rr < 16 ? 4 * cc + (rr >> 2) : 64 + 2 * cc + ((rr >> 2) & 1));
        const unsigned xres = __builtin_amdgcn_raw_buffer_load_b32(rx, od * 4, 0, 0);
        LDSWAIT(flag[7] == 0u, 2);
        u32x4 y[8];
        POLL8(y, WS_Y + rid * 8192, 0);
        float s0 = 0.f, s1 = 0.f;
#pragma unroll
        for (int i = 0; i < 8; ++i) { s0 += __uint_as_float(y[i][0]); s1 += __uint_as_float(y[i][2]); }
        float st = t32_sum(s0, s1);
        st = x16_sum(st); st += dpp<ROR4>(st); st += dpp<ROR8>(st);
        const auto sw = __builtin_amdgcn_permlane32_swap(__float_as_uint(st), __float_as_uint(st), false, false);
        if (lane < 4) reinterpret_cast<unsigned*>(out)[od] = pkbf(bflo(xres) + __uint_as_float(sw[0]), bfhi(xres) + __uint_as_float(sw[1]));
        if (rid == 0 && lane == 0) __builtin_amdgcn_raw_buffer_store_b32(epoch, rws, WS_EPOCH, 0, AUX_SC1);
      }
    }
  }
}
}

extern "C" void kernel_launch(void* const* d_in, const int* in_sizes, int n_in, void* d_out, int out_size, void* d_ws, size_t ws_size, hipStream_t stream) {
  using fk::u16;
  fk::fused<<<256, 512, 0, stream>>>((const u16*)d_in[0], (const u16*)d_in[1], (const u16*)d_in[2], (const u16*)d_in[3], (const u16*)d_in[4],
                                     (const u16*)d_in[6], (const u16*)d_in[7], (const u16*)d_in[8], (u16*)d_out, (unsigned char*)d_ws);
}
```

```cpp
#include <hip/hip_runtime.h>
#include <hip/hip_bf16.h>
#include <stdint.h>

namespace fk {
typedef unsigned short u16;
typedef unsigned u32x4 __attribute__((ext_vector_type(4)));
typedef unsigned u32x2 __attribute__((ext_vector_type(2)));
typedef unsigned u32x3 __attribute__((ext_vector_type(3)));
typedef float f32x4 __attribute__((ext_vector_type(4)));
typedef float f32x2 __attribute__((ext_vector_type(2)));
typedef __bf16 bf16x8 __attribute__((ext_vector_type(8)));
typedef __bf16 bf16x2 __attribute__((ext_vector_type(2)));
#define LAS __attribute__((address_space(3)))
#define SB() __builtin_amdgcn_sched_barrier(0)

constexpr int D = 576, SK = 32768, DH = 128, F = 4608, NQ = 4096;
#define P_POOL 8
constexpr int POOL = P_POOL;
#define P_CLAIM 4
constexpr int CLAIM = P_CLAIM;
#define P_XK 2
constexpr int XK = P_XK;
#define P_NEXTRA 4
constexpr int NEXTRA = P_NEXTRA;
#define P_PRE_T 12
constexpr int PRE_T = P_PRE_T;
#define P_FFN_T 3
constexpr int FFN_T = P_FFN_T;
constexpr int CODE_B = (P_NEXTRA == 4 ? 18 : 17) * 1024;
#define P_A 940
#define P_B 890
#define P_TB 56
#define P_OFF 175
#define WAUX 0
#define QAUX 0
constexpr int PACE1 = P_A, PACE2 = P_B, PACETB = P_TB, PACEOFF = P_OFF;
constexpr int CHUNK_B = 8192, PAIR_B = 16384;
constexpr int NP0 = (128 - POOL) / 2;
constexpr int NTK = 8 * POOL;
constexpr int GROUP_B = SK * DH * 2;
constexpr int NROW = 72;
static_assert(POOL % 2 == 0 && NTK <= 96 && NEXTRA % 2 == 0 && NP0 + XK < NROW && NP0 - 7 * XK >= 2 * (2 + NEXTRA), "");
constexpr int WS_EPOCH = 0, WS_CNT = 4096, WS_QP = 16384, WS_PART = WS_QP + 16 * 2 * 4 * 256 * 8, WS_ML = WS_PART + 32 * 8 * 256 * 8,
              WS_Y = WS_ML + 32 * 8 * 8 * 16, WS_END = WS_Y + 144 * 128 * 64;
constexpr int AUX_NT = 2, AUX_SC1 = 16, RSRC3 = 0x00020000, OOB = 0x7f000000;
constexpr unsigned SPIN_CAP = 1u << 18;
constexpr int L_XN = 0;
constexpr int L_QRED = L_XN + 2 * D;
constexpr int L_Q = L_QRED + 4 * 128;
constexpr int L_GU = L_Q + 128;
constexpr int L_H = L_GU + 2 * 3 * 64;
constexpr int L_ACC = L_H + 64;
constexpr int L_ML = L_ACC + 4 * 256;
constexpr int L_FLAG = L_ML + 16;
constexpr int L_DUMMY = L_FLAG + 16;
constexpr int L_LIST = L_DUMMY + 64;
constexpr int L_SC = L_LIST + 128;
constexpr int L_WORDS = L_SC + NROW * 64;
#define VMW(n) __builtin_amdgcn_s_waitcnt(0x0F70 | ((n) & 15) | (((n) >> 4) << 14))

__device__ __forceinline__ float bflo(unsigned v) { return __uint_as_float(v << 16); }
__device__ __forceinline__ float bfhi(unsigned v) { return __uint_as_float(v & 0xffff0000u); }
__device__ __forceinline__ unsigned pkbf(float lo, float hi) { bf16x2 r = {(__bf16)lo, (__bf16)hi}; return __builtin_bit_cast(unsigned, r); }
__device__ __forceinline__ float rbf(float f) { return (float)(__bf16)f; }
__device__ __forceinline__ float fdiv(float a, float d, float r) { const float q = a * r; return fmaf(fmaf(-q, d, a), r, q); }
template <int CTRL> __device__ __forceinline__ float dpp(float x) {
  return __builtin_bit_cast(float, __builtin_amdgcn_mov_dpp(__builtin_bit_cast(int, x), CTRL, 0xf, 0xf, true));
}
template <int CTRL> __device__ __forceinline__ unsigned dppu(unsigned x) { return (unsigned)__builtin_amdgcn_mov_dpp((int)x, CTRL, 0xf, 0xf, true); }
template <int CTRL, int BANK> __device__ __forceinline__ float dpp_bank(float old, float x) {
  return __builtin_bit_cast(float, __builtin_amdgcn_update_dpp(__builtin_bit_cast(int, old), __builtin_bit_cast(int, x), CTRL, 0xf, BANK, false));
}
constexpr int XOR1 = 0xB1, XOR2 = 0x4E, ROR4 = 0x124, ROR8 = 0x128, ROR12 = 0x12C, IDENT = 0xE4;
__device__ __forceinline__ float x16_max(float x) { auto s = __builtin_amdgcn_permlane16_swap(__float_as_uint(x), __float_as_uint(x), false, false); return fmaxf(__uint_as_float(s[0]), __uint_as_float(s[1])); }
__device__ __forceinline__ float x32_max(float x) { auto s = __builtin_amdgcn_permlane32_swap(__float_as_uint(x), __float_as_uint(x), false, false); return fmaxf(__uint_as_float(s[0]), __uint_as_float(s[1])); }
__device__ __forceinline__ float x16_sum(float x) { auto s = __builtin_amdgcn_permlane16_swap(__float_as_uint(x), __float_as_uint(x), false, false); return __uint_as_float(s[0]) + __uint_as_float(s[1]); }
__device__ __forceinline__ float x32_sum(float x) { auto s = __builtin_amdgcn_permlane32_swap(__float_as_uint(x), __float_as_uint(x), false, false); return __uint_as_float(s[0]) + __uint_as_float(s[1]); }
__device__ __forceinline__ float t32_sum(float a, float b) { auto s = __builtin_amdgcn_permlane32_swap(__float_as_uint(a), __float_as_uint(b), false, false); return __uint_as_float(s[0]) + __uint_as_float(s[1]); }
__device__ __forceinline__ float t16_sum(float a, float b) { auto s = __builtin_amdgcn_permlane16_swap(__float_as_uint(a), __float_as_uint(b), false, false); return __uint_as_float(s[0]) + __uint_as_float(s[1]); }
__device__ __forceinline__ f32x4 mfma(u32x4 a, u32x4 b, f32x4 c) {
  return __builtin_amdgcn_mfma_f32_16x16x32_bf16(__builtin_bit_cast(bf16x8, a), __builtin_bit_cast(bf16x8, b), c, 0, 0, 0);
}
__device__ __forceinline__ unsigned perm_lo(unsigned odd, unsigned even) { return __builtin_amdgcn_perm(odd, even, 0x05040100u); }
__device__ __forceinline__ unsigned perm_hi(unsigned odd, unsigned even) { return __builtin_amdgcn_perm(odd, even, 0x07060302u); }
typedef __amdgpu_buffer_rsrc_t rsrc_t;
__device__ __forceinline__ rsrc_t mkrs(const void* p, int bytes) { return __builtin_amdgcn_make_buffer_rsrc((void*)p, 0, bytes, RSRC3); }
#define LDSADD(idx, inc) __hip_atomic_fetch_add((LAS unsigned*)(LAS float*)(lds + (lane == 0 ? L_FLAG + (idx) : L_DUMMY + lane)), inc, __ATOMIC_RELAXED, __HIP_MEMORY_SCOPE_WORKGROUP)
#define LGKM0() asm volatile("s_waitcnt lgkmcnt(0)" ::: "memory")
#define CFENCE() asm volatile("" ::: "memory")
#define GRAN2(a, b) u32x4{__float_as_uint(a), epoch, __float_as_uint(b), epoch}
#define LDSWAIT(COND, SLP) for (unsigned spin = 0; (COND) && spin < (SPIN_CAP << 2); ++spin) __builtin_amdgcn_s_sleep(SLP)

__global__ void __launch_bounds__(512, 2) fused(const u16* __restrict__ x, const u16* __restrict__ kc, const u16* __restrict__ vc, const u16* __restrict__ wq,
                                                const u16* __restrict__ wo, const u16* __restrict__ wg, const u16* __restrict__ wu, const u16* __restrict__ wd,
                                                u16* out, unsigned char* ws) {
  __shared__ __attribute__((aligned(16))) float lds[L_WORDS];
  volatile LAS unsigned* flag = (volatile LAS unsigned*)(LAS float*)(lds + L_FLAG);
  volatile LAS unsigned* list = (volatile LAS unsigned*)(LAS float*)(lds + L_LIST);
  const int tid = threadIdx.x, lane = tid & 63, wave8 = __builtin_amdgcn_readfirstlane(tid >> 6), wave = wave8 & 3;
  const int g = blockIdx.x >> 3, s = blockIdx.x & 7, b = g >> 4, kvh = g & 15;
  const unsigned tstart = (unsigned)__builtin_amdgcn_s_memrealtime();
  const int s2 = kvh >> 1;
  const int NP = s == s2 ? NP0 - 7 * XK : NP0 + XK, LBASE = 2 * NP;
  constexpr float SCALE = 0.088388347648318440f, LOG2E = 1.4426950408889634f;
  unsigned long long pc0;
  asm volatile("s_getpc_b64 %0" : "=s"(pc0));
  const u32x4 cpf = __builtin_amdgcn_raw_buffer_load_b128(mkrs((const void*)(pc0 & ~255ull), CODE_B), lane * 16, wave8 == 4 ? g * 1024 : OOB, 0);
  SB();
  asm volatile("" :: "s"(x), "s"(kc), "s"(vc), "s"(wq), "s"(wo), "s"(wg), "s"(wu), "s"(wd), "s"(out), "s"(ws));
  const rsrc_t rws = mkrs(ws, WS_END);
  if (wave8 >= 4) {
    const u16* kb = kc + (size_t)g * (SK * DH); const u16* vb = vc + (size_t)g * (SK * DH);
    const int voffK = ((lane >> 2) & 3) * 256 + (lane >> 4) * 64 + (lane & 3) * 16, voffV = lane * 16;
    const rsrc_t rk = mkrs(kb, GROUP_B), rv = mkrs(vb, GROUP_B);
    const int obase = (s ^ (g >> 2)) << 20, xbase = ((s2 ^ (g >> 2)) << 20) + ((s - (s > s2)) * XK - 7 * XK) * PAIR_B;
    const int tsc = s == s2 ? (128 * 256) / (2 * (NP0 - 7 * XK) + POOL) : (128 * 256) / (2 * (NP0 + XK) + POOL);
#define POFF(p) (((p) < NP0 ? obase : xbase) + (p) * PAIR_B)
#define PACEWAIT(T) do { const int x_ = ((T) * tsc) >> 4; const unsigned due_ = (unsigned)(PACEOFF + ((min(x_, PACETB * 16) * PACE1 + max(x_ - PACETB * 16, 0) * PACE2) >> 8)); \
      for (unsigned spin = 0; (unsigned)__builtin_amdgcn_s_memrealtime() - tstart < due_ && spin < 4096u; ++spin) __builtin_amdgcn_s_sleep(2); } while (0)
    u32x4 R[2 + NEXTRA][8];
#define LD1(d, u, rs, vo, so) R[d][u] = __builtin_amdgcn_raw_buffer_load_b128(rs, (vo) + ((u) & 3) * 1024, (so) + ((u) >> 2) * 4096, AUX_NT)
#define PREFILL(LO, HI) _Pragma("unroll") for (int i = (LO); i < (HI); ++i) { if (i >= PRE_T) VMW(PRE_T - 1); \
      LD1(i >> 3, i & 7, rk, voffK, POFF((i >> 4) * 4 + wave) + ((i >> 3) & 1) * CHUNK_B); SB(); }
    PREFILL(0, 8)
    {
      const u32x4 sent = {~0u, ~0u, ~0u, ~0u}, ninf = {0xff80ff80u, 0xff80ff80u, 0xff80ff80u, 0xff80ff80u};
#pragma unroll
      for (int i = 0; i < 4; ++i) *reinterpret_cast<u32x4*>(lds + L_SC + i * 1024 + wave * 256 + lane * 4) = sent;
      if (wave < 2) *reinterpret_cast<u32x4*>(lds + L_SC + 4096 + wave * 256 + lane * 4) = (wave == 1 && lane >= 48) ? ninf : sent;
      else reinterpret_cast<unsigned*>(lds + L_LIST)[(wave - 2) * 64 + lane] = ~0u;
      LGKM0();
    }
    __builtin_amdgcn_s_barrier();
    SB();
    PREFILL(8, 8 * (2 + NEXTRA))
    asm volatile("" :: "v"(cpf));
    LDSWAIT(flag[1] == 0u, 1);
    CFENCE();
    const unsigned epoch = flag[0];
    const int n = lane & 15;
    const u32x4 qv = *reinterpret_cast<const u32x4*>(reinterpret_cast<const u16*>(lds + L_Q) + (n & 1) * 128 + (4 * (lane >> 4) + (n >> 2)) * 8);
    const u32x4 zero4 = {0u, 0u, 0u, 0u};
    const u32x4 Q0 = (n & 2) ? zero4 : qv, Q1 = (n & 2) ? qv : zero4;
    volatile LAS unsigned* scw = (volatile LAS unsigned*)(LAS float*)(lds + L_SC) + lane;
#define QK(d, rs, vo, so, SC, REFILL) do { \
      f32x4 dd[4]; \
      _Pragma("unroll") for (int p = 0; p < 4; ++p) { dd[p] = mfma(R[d][2 * p], Q0, f32x4{0.f, 0.f, 0.f, 0.f}); dd[p] = mfma(R[d][2 * p + 1], Q1, dd[p]); SB(); \
        if (REFILL) { LD1(d, 2 * p, rs, vo, so); SB(); LD1(d, 2 * p + 1, rs, vo, so); SB(); } } \
      float x0 = dpp<IDENT>(dd[0][0]), x1 = dpp<ROR12>(dd[0][1]), x2 = dpp<ROR8>(dd[0][2]), x3 = dpp<ROR4>(dd[0][3]); \
      x0 = dpp_bank<ROR4, 2>(x0, dd[1][0]); x1 = dpp_bank<IDENT, 2>(x1, dd[1][1]); x2 = dpp_bank<ROR12, 2>(x2, dd[1][2]); x3 = dpp_bank<ROR8, 2>(x3, dd[1][3]); \
      x0 = dpp_bank<ROR8, 4>(x0, dd[2][0]); x1 = dpp_bank<ROR4, 4>(x1, dd[2][1]); x2 = dpp_bank<IDENT, 4>(x2, dd[2][2]); x3 = dpp_bank<ROR12, 4>(x3, dd[2][3]); \
      x0 = dpp_bank<ROR12, 8>(x0, dd[3][0]); x1 = dpp_bank<ROR8, 8>(x1, dd[3][1]); x2 = dpp_bank<ROR4, 8>(x2, dd[3][2]); x3 = dpp_bank<IDENT, 8>(x3, dd[3][3]); \
      SC = rbf((x0 + x1) + (x2 + x3)) * SCALE; } while (0)
#define PV(d, rs, vo, so, SC) do { \
      const float pp = __builtin_amdgcn_exp2f(((SC) - m) * LOG2E); \
      l += pp; \
      const unsigned w_ = pkbf(pp, dpp<0x102>(pp)); \
      const u32x4 PA = {w_, dppu<0x104>(w_), dppu<0x108>(w_), dppu<0x10C>(w_)}; \
      u32x4 bf[8]; \
      SB(); \
      _Pragma("unroll") for (int t_ = 0; t_ < 4; ++t_) { \
        _Pragma("unroll") for (int c = 0; c < 4; ++c) { bf[2 * c][t_] = perm_lo(R[d][2 * t_ + 1][c], R[d][2 * t_][c]); bf[2 * c + 1][t_] = perm_hi(R[d][2 * t_ + 1][c], R[d][2 * t_][c]); } \
        SB(); LD1(d, 2 * t_, rs, vo, so); SB(); LD1(d, 2 * t_ + 1, rs, vo, so); SB(); } \
      _Pragma("unroll") for (int j = 0; j < 8; ++j) acc[j] = mfma(PA, bf[j], acc[j]); \
      SB(); } while (0)
#define RESCALE(SC) do { if (__any((SC) > m)) { \
        float pm = (SC); pm = fmaxf(pm, dpp<XOR2>(pm)); pm = fmaxf(pm, dpp<ROR4>(pm)); pm = fmaxf(pm, dpp<ROR8>(pm)); pm = x16_max(pm); pm = x32_max(pm); \
        const float mn = fmaxf(m, pm), al = __builtin_amdgcn_exp2f((m - mn) * LOG2E); \
        const float al0 = dpp<0x00>(al), al1 = dpp<0x55>(al); \
        l *= al; m = mn; \
        _Pragma("unroll") for (int j = 0; j < 8; ++j) { acc[j][0] *= al0; acc[j][1] *= al1; } } } while (0)
#define POOLITEM(SO) do { const int li = min(t - LBASE, 127); \
        unsigned e = (unsigned)__builtin_amdgcn_readfirstlane((int)list[li]); \
        for (unsigned spin = 0; e == ~0u && spin < (SPIN_CAP << 2); ++spin) e = (unsigned)__builtin_amdgcn_readfirstlane((int)list[li]); \
        SO = e < 1024u ? (int)e * CHUNK_B : GROUP_B; } while (0)
    float m = -3.0e38f;
    int cp = wave, t;
    unsigned lt = LDSADD(8, 1u);
    bool first = true;
    do {
      t = __builtin_amdgcn_readfirstlane((int)lt);
      lt = LDSADD(8, t < LBASE ? 1u : 0u);
      const bool nk = t < NP;
      const int np = nk ? t : (t < LBASE ? t - NP : NROW - 1);
      const rsrc_t rs = mkrs(nk ? kb : vb, GROUP_B);
      const int vo = nk ? voffK : voffV, so = np < NROW - 1 ? POFF(np) : GROUP_B;
      PACEWAIT(t);
      SB();
      { float s0, s1; QK(0, rs, vo, so, s0, true); QK(1, rs, vo, so + CHUNK_B, s1, true);
        const unsigned pk = min(pkbf(s0, s1), 0xfffffffeu); scw[cp * 64] = pk; m = fmaxf(fmaxf(m, bflo(pk)), bfhi(pk)); SB(); }
      if (first) {
#pragma unroll
        for (int d = 2; d < 2 + NEXTRA; d += 2) { float s0, s1; QK(d, rs, 0, 0, s0, false); QK(d + 1, rs, 0, 0, s1, false);
          const unsigned pk = min(pkbf(s0, s1), 0xfffffffeu); scw[((d >> 1) * 4 + wave) * 64] = pk; m = fmaxf(fmaxf(m, bflo(pk)), bfhi(pk)); SB(); }
        first = false;
      }
      cp = np;
    } while (t < NP);
    if (t >= LBASE) lt = (unsigned)t;
    m = fmaxf(m, dpp<XOR2>(m)); m = fmaxf(m, dpp<ROR4>(m)); m = fmaxf(m, dpp<ROR8>(m)); m = x16_max(m); m = x32_max(m);
    f32x4 acc[8];
#pragma unroll
    for (int j = 0; j < 8; ++j) acc[j] = f32x4{0.f, 0.f, 0.f, 0.f};
    float l = 0.f;
    int so;
    do {
      t = __builtin_amdgcn_readfirstlane((int)lt);
      lt = LDSADD(8, 1u);
      unsigned pk = scw[cp * 64];
      const bool nv = t < LBASE;
      so = POFF(t - NP);
      if (!nv) POOLITEM(so);
      const rsrc_t rs0 = mkrs(nv ? vb : kb, GROUP_B);
      const int vo0 = nv ? voffV : voffK, so1 = nv ? so + CHUNK_B : so;
      for (unsigned spin = 0; __any(pk == ~0u) && spin < (SPIN_CAP << 2); ++spin) pk = scw[cp * 64];
      const float sc0 = bflo(pk), sc1 = bfhi(pk), scm = fmaxf(sc0, sc1);
      RESCALE(scm);
      PACEWAIT(t);
      SB();
      PV(0, rs0, vo0, so, sc0);
      PV(1, rv, voffV, so1, sc1);
      cp = t - NP;
    } while (t < LBASE);
    while (so < GROUP_B) {
      t = __builtin_amdgcn_readfirstlane((int)lt);
      lt = LDSADD(8, 1u);
      POOLITEM(so);
      SB();
      float sc;
      QK(0, rk, voffK, so, sc, true);
      sc = rbf(sc);
      RESCALE(sc);
      PV(1, rv, voffV, so, sc);
    }
    l += dpp<XOR2>(l); l += dpp<ROR4>(l); l += dpp<ROR8>(l); l = x16_sum(l); l = x32_sum(l);
    {
      volatile LAS f32x4* aw = (volatile LAS f32x4*)(LAS float*)(lds + L_ACC + wave * 256 + lane * 8);
      if (lane < 16) {
#pragma unroll
        for (int h = 0; h < 2; ++h) {
          aw[h * 32] = f32x4{acc[0][h], acc[1][h], acc[2][h], acc[3][h]};
          aw[h * 32 + 1] = f32x4{acc[4][h], acc[5][h], acc[6][h], acc[7][h]};
        }
        if (lane < 2) *(volatile LAS f32x2*)(LAS float*)(lds + L_ML + (wave * 2 + lane) * 2) = f32x2{m, l};
      }
    }
    const unsigned old = LDSADD(2, 1u);
    CFENCE();
    f32x2 mlw[4]; f32x4 av[4];
#pragma unroll
    for (int w = 0; w < 4; ++w) {
      mlw[w] = *(volatile LAS f32x2*)(LAS float*)(lds + L_ML + w * 4 + (lane >> 5) * 2);
      av[w] = *(volatile LAS f32x4*)(LAS float*)(lds + L_ACC + w * 256 + lane * 4);
    }
    const int po = WS_PART + (g * 8 + s) * 2048 + lane * 16, mo = (lane & 7) == 0 ? WS_ML + g * 1024 + (lane >> 3) * 128 + s * 16 : OOB;
    if (__builtin_amdgcn_readfirstlane(old) == 3u) {
      const float M = fmaxf(fmaxf(mlw[0][0], mlw[1][0]), fmaxf(mlw[2][0], mlw[3][0]));
      float L = 0.f; f32x4 o = {0.f, 0.f, 0.f, 0.f};
#pragma unroll
      for (int w = 0; w < 4; ++w) {
        const float wt = __builtin_amdgcn_exp2f((mlw[w][0] - M) * LOG2E);
        L = fmaf(wt, mlw[w][1], L); o += wt * av[w];
      }
      __builtin_amdgcn_raw_buffer_store_b128(GRAN2(o[0], o[1]), rws, po, 0, AUX_SC1);
      __builtin_amdgcn_raw_buffer_store_b128(GRAN2(o[2], o[3]), rws, po + 1024, 0, AUX_SC1);
      __builtin_amdgcn_raw_buffer_store_b128(GRAN2(M, L), rws, mo, 0, AUX_SC1);
      flag[3] = 1u;
    }
  } else {
    __builtin_amdgcn_s_setprio(3);
    const int hw = wave;
    const rsrc_t rwq = mkrs(wq, D * NQ * 2), rx = mkrs(x, 2 * D * 2);
    const int cb = s & 3, rq = (s >> 2) + 2 * b;
    const int qrow0 = 144 * rq + 36 * hw + (lane >> 3);
    const unsigned epoch = __builtin_amdgcn_raw_buffer_load_b32(rws, WS_EPOCH, 0, AUX_SC1) + 1u;
    SB();
    u32x3 xr[3];
#pragma unroll
    for (int i = 0; i < 3; ++i) xr[i] = __builtin_amdgcn_raw_buffer_load_b96(rx, lane * 36 + 12 * i, 0, 0);
    u32x4 wqr[5];
#pragma unroll
    for (int i = 0; i < 5; ++i) wqr[i] = __builtin_amdgcn_raw_buffer_load_b128(rwq, (i == 4 && lane >= 32) ? OOB : (qrow0 + 8 * i) * (NQ * 2) + (kvh * 256 + cb * 64 + (lane & 7) * 8) * 2, 0, QAUX);
    if (tid < 16) flag[lane] = lane == 8 ? 2u * (2 + NEXTRA) : 0u;
    LGKM0();
    SB();
    __builtin_amdgcn_s_barrier();
    SB();
    const int gq = lane >> 4, c = lane & 15;
    {
      float v[18];
#pragma unroll
      for (int i = 0; i < 3; ++i)
#pragma unroll
        for (int j = 0; j < 3; ++j) { v[6 * i + 2 * j] = bflo(xr[i][j]); v[6 * i + 2 * j + 1] = bfhi(xr[i][j]); }
      float sm = 0.f;
#pragma unroll
      for (int i = 0; i < 18; ++i) sm += v[i];
      sm += dpp<XOR1>(sm); sm += dpp<XOR2>(sm); sm += dpp<ROR4>(sm); sm += dpp<ROR8>(sm); sm = x16_sum(sm);
      const float mean = sm * (1.f / D);
      float ss = 0.f;
#pragma unroll
      for (int i = 0; i < 18; ++i) { const float d = v[i] - mean; ss = fmaf(d, d, ss); }
      ss += dpp<XOR1>(ss); ss += dpp<XOR2>(ss); ss += dpp<ROR4>(ss); ss += dpp<ROR8>(ss); ss = x16_sum(ss);
      const float mb = rbf(mean), den = rbf(sqrtf(rbf(rbf(ss * (1.f / D)) + 1e-5f))), rden = 1.f / den;
#pragma unroll
      for (int i = 0; i < 18; i += 2) {
        float2 o; o.x = rbf(fdiv(rbf(v[i] - mb), den, rden)); o.y = rbf(fdiv(rbf(v[i + 1] - mb), den, rden));
        *reinterpret_cast<float2*>(lds + L_XN + lane * 18 + i) = o;
      }
      LGKM0();
      float a0[8], a1[8];
#pragma unroll
      for (int j = 0; j < 8; ++j) { a0[j] = 0.f; a1[j] = 0.f; }
#pragma unroll
      for (int i = 0; i < 5; ++i) {
        const int rr = i == 4 ? min(qrow0 + 32, D - 1) : qrow0 + 8 * i;
        const float x0 = lds[L_XN + rr], x1 = lds[L_XN + D + rr];
#pragma unroll
        for (int j = 0; j < 4; ++j) {
          const float lo = bflo(wqr[i][j]), hi = bfhi(wqr[i][j]);
          a0[2 * j] = fmaf(x0, lo, a0[2 * j]); a0[2 * j + 1] = fmaf(x0, hi, a0[2 * j + 1]);
          a1[2 * j] = fmaf(x1, lo, a1[2 * j]); a1[2 * j + 1] = fmaf(x1, hi, a1[2 * j + 1]);
        }
      }
      float r1[8], r2[4];
#pragma unroll
      for (int j = 0; j < 8; ++j) r1[j] = t32_sum(a0[j], a1[j]);
#pragma unroll
      for (int j = 0; j < 4; ++j) { r2[j] = t16_sum(r1[j], r1[4 + j]); r2[j] += dpp<ROR8>(r2[j]); }
      if ((lane & 8) == 0) *reinterpret_cast<f32x4*>(lds + L_QRED + hw * 128 + (lane >> 5) * 64 + (lane & 7) * 8 + ((lane >> 4) & 1) * 4) = f32x4{r2[0], r2[1], r2[2], r2[3]};
      CFENCE();
      (void)LDSADD(6, 1u);
    }
    if (hw) {
      const rsrc_t rwg = mkrs(wg, D * F * 2), rwu = mkrs(wu, D * F * 2), rwd = mkrs(wd, F * D * 2), rwo = mkrs(wo, NQ * D * 2);
      const int colA = (hw - 1) * 192 + 8 * c, colB = (hw - 1) * 192 + 128 + 4 * c;
      const int frow0 = 192 * (hw - 1) + (lane >> 2);
      u32x4 gw[12], uw[12], wa[8], fr[12]; u32x2 wb[8];
#define LDW(rs, row0) _Pragma("unroll") for (int j = 0; j < 8; ++j) { \
      VMW(FFN_T - 1); wa[j] = __builtin_amdgcn_raw_buffer_load_b128(rs, (8 * gq + j) * (D * 2) + colA * 2, (row0) * (D * 2), WAUX); SB(); \
      VMW(FFN_T - 1); wb[j] = __builtin_amdgcn_raw_buffer_load_b64(rs, (8 * gq + j) * (D * 2) + colB * 2, (row0) * (D * 2), WAUX); SB(); } \
      _Pragma("unroll") for (int cc = 0; cc < 4; ++cc) { \
        fr[2 * cc] = u32x4{perm_lo(wa[1][cc], wa[0][cc]), perm_lo(wa[3][cc], wa[2][cc]), perm_lo(wa[5][cc], wa[4][cc]), perm_lo(wa[7][cc], wa[6][cc])}; \
        fr[2 * cc + 1] = u32x4{perm_hi(wa[1][cc], wa[0][cc]), perm_hi(wa[3][cc], wa[2][cc]), perm_hi(wa[5][cc], wa[4][cc]), perm_hi(wa[7][cc], wa[6][cc])}; } \
      _Pragma("unroll") for (int cc = 0; cc < 2; ++cc) { \
        fr[8 + 2 * cc] = u32x4{perm_lo(wb[1][cc], wb[0][cc]), perm_lo(wb[3][cc], wb[2][cc]), perm_lo(wb[5][cc], wb[4][cc]), perm_lo(wb[7][cc], wb[6][cc])}; \
        fr[9 + 2 * cc] = u32x4{perm_hi(wb[1][cc], wb[0][cc]), perm_hi(wb[3][cc], wb[2][cc]), perm_hi(wb[5][cc], wb[4][cc]), perm_hi(wb[7][cc], wb[6][cc])}; }
#define YMFMA(AF) do { \
      f32x4 ya[12]; const f32x4 z4 = {0.f, 0.f, 0.f, 0.f}; \
      _Pragma("unroll") for (int j = 0; j < 12; ++j) ya[j] = mfma(AF, fr[j], z4); \
      const float a0_ = (gq & 1) ? ya[2][0] : ya[0][0], a1_ = (gq & 1) ? ya[6][0] : ya[4][0]; e0 += (gq & 2) ? a1_ : a0_; \
      const float b0_ = (gq & 1) ? ya[3][0] : ya[1][0], b1_ = (gq & 1) ? ya[7][0] : ya[5][0]; e1 += (gq & 2) ? b1_ : b0_; \
      e2 += (gq & 1) ? ya[10][0] : ya[8][0]; e3 += (gq & 1) ? ya[11][0] : ya[9][0]; } while (0)
      float e0 = 0.f, e1 = 0.f, e2 = 0.f, e3 = 0.f;
      const int nunit = s == s2 ? 2 : 1;
#pragma unroll 1
      for (int it = 0; it < nunit; ++it) {
        const int jl = s * 8 + (kvh >> 1), jr = jl % 7;
        const int line = it ? s * 8 + 3 : (jl == 63 ? 67 : (jl / 7) * 8 + jr + (jr >= 3));
        const int u0 = line * 64 + (kvh & 1) * 32;
#pragma unroll
        for (int i = 0; i < 12; ++i) {
          VMW(FFN_T - 1); gw[i] = __builtin_amdgcn_raw_buffer_load_b128(rwg, frow0 * (F * 2) + (lane & 3) * 16, 16 * i * (F * 2) + u0 * 2, WAUX); SB();
          VMW(FFN_T - 1); uw[i] = __builtin_amdgcn_raw_buffer_load_b128(rwu, frow0 * (F * 2) + (lane & 3) * 16, 16 * i * (F * 2) + u0 * 2, WAUX); SB();
        }
        float ag[8], au[8];
#pragma unroll
        for (int j = 0; j < 8; ++j) { ag[j] = 0.f; au[j] = 0.f; }
#pragma unroll
        for (int i = 0; i < 12; ++i) {
          const float xv = lds[L_XN + b * D + frow0 + 16 * i];
#pragma unroll
          for (int j = 0; j < 4; ++j) {
            ag[2 * j] = fmaf(xv, bflo(gw[i][j]), ag[2 * j]); ag[2 * j + 1] = fmaf(xv, bfhi(gw[i][j]), ag[2 * j + 1]);
            au[2 * j] = fmaf(xv, bflo(uw[i][j]), au[2 * j]); au[2 * j + 1] = fmaf(xv, bfhi(uw[i][j]), au[2 * j + 1]);
          }
        }
        float r1[8], r2[4];
#pragma unroll
        for (int j = 0; j < 8; ++j) r1[j] = t32_sum(ag[j], au[j]);
#pragma unroll
        for (int j = 0; j < 4; ++j) { r2[j] = t16_sum(r1[j], r1[4 + j]); r2[j] += dpp<ROR4>(r2[j]); r2[j] += dpp<ROR8>(r2[j]); }
        if ((lane & 12) == 0) *reinterpret_cast<f32x4*>(lds + L_GU + (it * 3 + hw - 1) * 64 + (lane >> 5) * 32 + (lane & 3) * 8 + ((lane >> 4) & 1) * 4) = f32x4{r2[0], r2[1], r2[2], r2[3]};
        CFENCE();
        (void)LDSADD(4 + it, 1u);
        LDW(rwd, u0)
        LDSWAIT(flag[4 + it] < 3u, 1);
        CFENCE();
        const int cc = lane & 31;
        const float* gu = lds + L_GU + it * 192 + cc;
        const float gt = rbf((gu[0] + gu[64]) + gu[128]), up = rbf((gu[32] + gu[96]) + gu[160]);
        const float y2 = -2.f * 0.7978845608028654f * LOG2E * (gt + 0.044715f * gt * gt * gt);
        const float ge = rbf(gt / (1.f + __builtin_amdgcn_exp2f(y2)));
        reinterpret_cast<__bf16*>(lds + L_H + hw * 16)[cc] = (__bf16)(ge * up);
        LGKM0();
        const u32x4 HA = *reinterpret_cast<const u32x4*>(lds + L_H + hw * 16 + gq * 4);
        YMFMA(HA);
        SB();
      }
      LDW(rwo, kvh * 256 + s * 32)
      const int sp = (lane >> 1) & 7;
      int po = WS_PART + (g * 8 + sp) * 2048 + (s * 8 + gq * 2 + (lane & 1)) * 16, mo = WS_ML + g * 1024 + s * 128 + sp * 16;
      int yoA = WS_Y + ((b * 72 + (hw - 1) * 24 + (lane >> 2)) * 128 + kvh * 8 + s) * 64 + (lane & 3) * 16, yoB = lane < 32 ? yoA + 16 * 8192 : OOB;
#pragma unroll
      for (int j = 0; j < 12; ++j) asm volatile("" : "+v"(fr[j]));
      asm volatile("" : "+v"(e0), "+v"(e1), "+v"(e2), "+v"(e3), "+v"(po), "+v"(mo), "+v"(yoA), "+v"(yoB));
      LDSWAIT((int)flag[8] < LBASE - 6, 32);
      if (hw == 1) {
        const u32x4 w0 = __builtin_amdgcn_raw_buffer_load_b128(rws, po, 0, AUX_SC1), w1 = __builtin_amdgcn_raw_buffer_load_b128(rws, po + 1024, 0, AUX_SC1), w2 = __builtin_amdgcn_raw_buffer_load_b128(rws, mo, 0, AUX_SC1);
        const u32x4 w3 = __builtin_amdgcn_raw_buffer_load_b128(mkrs((const void*)(pc0 & ~255ull), CODE_B + 512), lane * 16, g * 1024, 0);
        asm volatile("" :: "v"(w0), "v"(w1), "v"(w2), "v"(w3));
      } else {
        u32x4 w[4];
#pragma unroll
        for (int r_ = 0; r_ < 4; ++r_) w[r_] = __builtin_amdgcn_raw_buffer_load_b128(rws, lane * 16 + r_ * 1024, blockIdx.x < 144 ? WS_Y + blockIdx.x * 8192 + (hw - 2) * 4096 : OOB, AUX_SC1);
        asm volatile("" :: "v"(w[0]), "v"(w[1]), "v"(w[2]), "v"(w[3]));
      }
      LDSWAIT(flag[3] == 0u, 2);
      u32x4 p0, p1, ml;
      for (unsigned spin = 0;; ++spin) {
        CFENCE();
        p0 = __builtin_amdgcn_raw_buffer_load_b128(rws, po, 0, AUX_SC1);
        p1 = __builtin_amdgcn_raw_buffer_load_b128(rws, po + 1024, 0, AUX_SC1);
        ml = __builtin_amdgcn_raw_buffer_load_b128(rws, mo, 0, AUX_SC1);
        const bool ok = (((p0[1] ^ epoch) | (p0[3] ^ epoch) | (p1[1] ^ epoch)) | ((p1[3] ^ epoch) | (ml[1] ^ epoch) | (ml[3] ^ epoch))) == 0u;
        if (__all(ok) || spin > SPIN_CAP) break;
      }
      const float ms = __uint_as_float(ml[0]);
      float M = ms; M = fmaxf(M, dpp<XOR2>(M)); M = fmaxf(M, dpp<ROR4>(M)); M = fmaxf(M, dpp<ROR8>(M));
      const float wt = __builtin_amdgcn_exp2f((ms - M) * LOG2E);
      float v[5] = {wt * __uint_as_float(p0[0]), wt * __uint_as_float(p0[2]), wt * __uint_as_float(p1[0]), wt * __uint_as_float(p1[2]), wt * __uint_as_float(ml[2])};
#pragma unroll
      for (int j = 0; j < 5; ++j) { v[j] += dpp<XOR2>(v[j]); v[j] += dpp<ROR4>(v[j]); v[j] += dpp<ROR8>(v[j]); }
      const float r0 = __builtin_amdgcn_rcpf(v[4]), inv = fmaf(fmaf(-v[4], r0, 1.f), r0, r0);
      const unsigned o0 = pkbf(v[0] * inv, v[1] * inv), o1 = pkbf(v[2] * inv, v[3] * inv);
      const u32x4 OA = {dppu<0xA0>(o0), dppu<0xA0>(o1), dppu<0xF5>(o0), dppu<0xF5>(o1)};
      YMFMA(OA);
      __builtin_amdgcn_raw_buffer_store_b128(GRAN2(e0, e1), rws, yoA, 0, AUX_SC1);
      __builtin_amdgcn_raw_buffer_store_b128(GRAN2(e2, e3), rws, yoB, 0, AUX_SC1);
      flag[7] = 1u;
    } else {
      for (unsigned spin = 0; flag[6] < 4u && spin < (SPIN_CAP << 2); ++spin) {}
      CFENCE();
      {
        const float2 q0 = *reinterpret_cast<const float2*>(lds + L_QRED + 2 * lane), q1 = *reinterpret_cast<const float2*>(lds + L_QRED + 128 + 2 * lane);
        const float2 q2 = *reinterpret_cast<const float2*>(lds + L_QRED + 256 + 2 * lane), q3 = *reinterpret_cast<const float2*>(lds + L_QRED + 384 + 2 * lane);
        const float qa = (q0.x + q1.x) + (q2.x + q3.x), qb = (q0.y + q1.y) + (q2.y + q3.y);
        __builtin_amdgcn_raw_buffer_store_b128(GRAN2(qa, qb), rws, WS_QP + (((kvh * 2 + (lane >> 5)) * 4 + rq) * 256 + cb * 64 + 2 * (lane & 31)) * 8, 0, AUX_SC1);
      }
#define POLL8(P, base, SLP) do { for (unsigned spin = 0;; ++spin) { CFENCE(); if (SLP) __builtin_amdgcn_s_sleep(SLP); \
        _Pragma("unroll") for (int r_ = 0; r_ < 8; ++r_) P[r_] = __builtin_amdgcn_raw_buffer_load_b128(rws, lane * 16 + (r_ & 3) * 1024, (base) + (r_ >> 2) * 4096, AUX_SC1); \
        unsigned bad_ = 0; _Pragma("unroll") for (int r_ = 0; r_ < 8; ++r_) bad_ |= (P[r_][1] ^ epoch) | (P[r_][3] ^ epoch); \
        if (__all(bad_ == 0u) || spin > SPIN_CAP) break; } } while (0)
      {
        u32x4 p[8];
        __builtin_amdgcn_s_sleep(24);
        POLL8(p, WS_QP + (kvh * 2 + b) * 8192, 8);
#pragma unroll
        for (int hh = 0; hh < 2; ++hh) {
          const float qa = (__uint_as_float(p[hh][0]) + __uint_as_float(p[2 + hh][0])) + (__uint_as_float(p[4 + hh][0]) + __uint_as_float(p[6 + hh][0]));
          const float qb = (__uint_as_float(p[hh][2]) + __uint_as_float(p[2 + hh][2])) + (__uint_as_float(p[4 + hh][2]) + __uint_as_float(p[6 + hh][2]));
          reinterpret_cast<unsigned*>(lds + L_Q)[hh * 64 + lane] = pkbf(qa, qb);
        }
      }
      flag[0] = epoch;
      CFENCE();
      flag[1] = 1u;
      if (s == 0) {
        __builtin_amdgcn_raw_buffer_store_b32(0u, rws, lane == 0 ? WS_CNT + g * 256 : OOB, 0, AUX_SC1);
        asm volatile("s_waitcnt vmcnt(0)" ::: "memory");
        __builtin_amdgcn_raw_buffer_store_b32(epoch, rws, lane == 0 ? WS_CNT + g * 256 + 128 : OOB, 0, AUX_SC1);
      }
      LDSWAIT((int)flag[8] < LBASE - 24, 32);
      for (unsigned spin = 0;; ++spin) {
        CFENCE();
        const unsigned f = __builtin_amdgcn_raw_buffer_load_b32(rws, WS_CNT + g * 256 + 128, 0, AUX_SC1);
        if (__all(f == epoch) || spin > SPIN_CAP) break;
        __builtin_amdgcn_s_sleep(8);
      }
      {
        const rsrc_t rc = mkrs(ws + WS_CNT + g * 256, 4);
        int req = 0;
        for (unsigned spin = 0; spin < (SPIN_CAP << 2); ++spin) {
          if ((int)__builtin_amdgcn_readfirstlane((int)flag[8]) - LBASE <= req) { __builtin_amdgcn_s_sleep(2); continue; }
          const unsigned t0 = (unsigned)__builtin_amdgcn_readfirstlane(__builtin_amdgcn_raw_ptr_buffer_atomic_add_i32(CLAIM, rc, lane == 0 ? 0 : 64, 0, 0));
          const int got = t0 >= (unsigned)NTK ? 0 : min(CLAIM, NTK - (int)t0);
          const unsigned tt = t0 + (unsigned)lane;
          if (lane < got) list[req + lane] = (tt & 7) * 128 + 2 * NP0 + (tt >> 3);
          req += got;
          if (got < CLAIM || (int)t0 + CLAIM >= NTK || req >= NTK) break;
        }
        if (lane >= req) list[lane] = 0x7fffffffu;
        if (lane + 64 >= req) list[lane + 64] = 0x7fffffffu;
      }
      if (blockIdx.x < 144) {
        const int rid = blockIdx.x, rb = rid % 72, rw = rb / 24, rr = rb % 24;
        const int cc = (rr & 3) * 4 + (lane & 3);
        const int od = (rid / 72) * (D / 2) + rw * 96 + (rr < 16 ? 4 * cc + (rr >> 2) : 64 + 2 * cc + ((rr >> 2) & 1));
        const unsigned xres = __builtin_amdgcn_raw_buffer_load_b32(rx, od * 4, 0, 0);
        LDSWAIT(flag[7] == 0u, 2);
        u32x4 y[8];
        POLL8(y, WS_Y + rid * 8192, 0);
        float s0 = 0.f, s1 = 0.f;
#pragma unroll
        for (int i = 0; i < 8; ++i) { s0 += __uint_as_float(y[i][0]); s1 += __uint_as_float(y[i][2]); }
        float st = t32_sum(s0, s1);
        st = x16_sum(st); st += dpp<ROR4>(st); st += dpp<ROR8>(st);
        const auto sw = __builtin_amdgcn_permlane32_swap(__float_as_uint(st), __float_as_uint(st), false, false);
        if (lane < 4) reinterpret_cast<unsigned*>(out)[od] = pkbf(bflo(xres) + __uint_as_float(sw[0]), bfhi(xres) + __uint_as_float(sw[1]));
        if (rid == 0 && lane == 0) __builtin_amdgcn_raw_buffer_store_b32(epoch, rws, WS_EPOCH, 0, AUX_SC1);
      }
    }
  }
}
}

extern "C" void kernel_launch(void* const* d_in, const int* in_sizes, int n_in, void* d_out, int out_size, void* d_ws, size_t ws_size, hipStream_t stream) {
  using fk::u16;
  fk::fused<<<256, 512, 0, stream>>>((const u16*)d_in[0], (const u16*)d_in[1], (const u16*)d_in[2], (const u16*)d_in[3], (const u16*)d_in[4],
                                     (const u16*)d_in[6], (const u16*)d_in[7], (const u16*)d_in[8], (u16*)d_out, (unsigned char*)d_ws);
}
```

```cpp
#include <hip/hip_runtime.h>
#include <hip/hip_bf16.h>
#include <stdint.h>

namespace fk {
typedef unsigned short u16;
typedef unsigned u32x4 __attribute__((ext_vector_type(4)));
typedef unsigned u32x2 __attribute__((ext_vector_type(2)));
typedef unsigned u32x3 __attribute__((ext_vector_type(3)));
typedef float f32x4 __attribute__((ext_vector_type(4)));
typedef float f32x2 __attribute__((ext_vector_type(2)));
typedef __bf16 bf16x8 __attribute__((ext_vector_type(8)));
typedef __bf16 bf16x2 __attribute__((ext_vector_type(2)));
#define LAS __attribute__((address_space(3)))
#define SB() __builtin_amdgcn_sched_barrier(0)

constexpr int D = 576, SK = 32768, DH = 128, F = 4608, NQ = 4096;
constexpr int POOL = 8;
constexpr int CLAIM = 4;
constexpr int XK = 2;
constexpr int NEXTRA = 4;
constexpr int PRE_T = 12;
constexpr int FFN_T = 3;
constexpr int CODE_B = 18 * 1024;
constexpr int PACE1 = 940, PACE2 = 890, PACETB = 56, PACEOFF = 175;
constexpr int CHUNK_B = 8192, PAIR_B = 16384;
constexpr int NP0 = (128 - POOL) / 2;
constexpr int NTK = 8 * POOL;
constexpr int GROUP_B = SK * DH * 2;
constexpr int NROW = 72;
static_assert(POOL % 2 == 0 && NTK <= 96 && NEXTRA % 2 == 0 && NP0 + XK < NROW && NP0 - 7 * XK >= 2 * (2 + NEXTRA), "");
constexpr int WS_EPOCH = 0, WS_CNT = 4096, WS_QP = 16384, WS_PART = WS_QP + 16 * 2 * 4 * 256 * 8, WS_ML = WS_PART + 32 * 8 * 256 * 8,
              WS_Y = WS_ML + 32 * 8 * 8 * 16, WS_END = WS_Y + 144 * 128 * 64;
constexpr int AUX_NT = 2, AUX_SC1 = 16, RSRC3 = 0x00020000, OOB = 0x7f000000;
constexpr unsigned SPIN_CAP = 1u << 18;
constexpr int L_XN = 0;
constexpr int L_QRED = L_XN + 2 * D;
constexpr int L_Q = L_QRED + 4 * 128;
constexpr int L_GU = L_Q + 128;
constexpr int L_H = L_GU + 2 * 3 * 64;
constexpr int L_ACC = L_H + 64;
constexpr int L_ML = L_ACC + 4 * 256;
constexpr int L_FLAG = L_ML + 16;
constexpr int L_DUMMY = L_FLAG + 16;
constexpr int L_LIST = L_DUMMY + 64;
constexpr int L_SC = L_LIST + 128;
constexpr int L_WORDS = L_SC + NROW * 64;
#define VMW(n) __builtin_amdgcn_s_waitcnt(0x0F70 | ((n) & 15) | (((n) >> 4) << 14))

__device__ __forceinline__ float bflo(unsigned v) { return __uint_as_float(v << 16); }
__device__ __forceinline__ float bfhi(unsigned v) { return __uint_as_float(v & 0xffff0000u); }
__device__ __forceinline__ unsigned pkbf(float lo, float hi) { bf16x2 r = {(__bf16)lo, (__bf16)hi}; return __builtin_bit_cast(unsigned, r); }
__device__ __forceinline__ float rbf(float f) { return (float)(__bf16)f; }
__device__ __forceinline__ float fdiv(float a, float d, float r) { const float q = a * r; return fmaf(fmaf(-q, d, a), r, q); }
template <int CTRL> __device__ __forceinline__ float dpp(float x) {
  return __builtin_bit_cast(float, __builtin_amdgcn_mov_dpp(__builtin_bit_cast(int, x), CTRL, 0xf, 0xf, true));
}
template <int CTRL> __device__ __forceinline__ unsigned dppu(unsigned x) { return (unsigned)__builtin_amdgcn_mov_dpp((int)x, CTRL, 0xf, 0xf, true); }
template <int CTRL, int BANK> __device__ __forceinline__ float dpp_bank(float old, float x) {
  return __builtin_bit_cast(float, __builtin_amdgcn_update_dpp(__builtin_bit_cast(int, old), __builtin_bit_cast(int, x), CTRL, 0xf, BANK, false));
}
constexpr int XOR1 = 0xB1, XOR2 = 0x4E, ROR4 = 0x124, ROR8 = 0x128, ROR12 = 0x12C, IDENT = 0xE4;
__device__ __forceinline__ float x16_max(float x) { auto s = __builtin_amdgcn_permlane16_swap(__float_as_uint(x), __float_as_uint(x), false, false); return fmaxf(__uint_as_float(s[0]), __uint_as_float(s[1])); }
__device__ __forceinline__ float x32_max(float x) { auto s = __builtin_amdgcn_permlane32_swap(__float_as_uint(x), __float_as_uint(x), false, false); return fmaxf(__uint_as_float(s[0]), __uint_as_float(s[1])); }
__device__ __forceinline__ float x16_sum(float x) { auto s = __builtin_amdgcn_permlane16_swap(__float_as_uint(x), __float_as_uint(x), false, false); return __uint_as_float(s[0]) + __uint_as_float(s[1]); }
__device__ __forceinline__ float x32_sum(float x) { auto s = __builtin_amdgcn_permlane32_swap(__float_as_uint(x), __float_as_uint(x), false, false); return __uint_as_float(s[0]) + __uint_as_float(s[1]); }
__device__ __forceinline__ float t32_sum(float a, float b) { auto s = __builtin_amdgcn_permlane32_swap(__float_as_uint(a), __float_as_uint(b), false, false); return __uint_as_float(s[0]) + __uint_as_float(s[1]); }
__device__ __forceinline__ float t16_sum(float a, float b) { auto s = __builtin_amdgcn_permlane16_swap(__float_as_uint(a), __float_as_uint(b), false, false); return __uint_as_float(s[0]) + __uint_as_float(s[1]); }
__device__ __forceinline__ f32x4 mfma(u32x4 a, u32x4 b, f32x4 c) {
  return __builtin_amdgcn_mfma_f32_16x16x32_bf16(__builtin_bit_cast(bf16x8, a), __builtin_bit_cast(bf16x8, b), c, 0, 0, 0);
}
__device__ __forceinline__ unsigned perm_lo(unsigned odd, unsigned even) { return __builtin_amdgcn_perm(odd, even, 0x05040100u); }
__device__ __forceinline__ unsigned perm_hi(unsigned odd, unsigned even) { return __builtin_amdgcn_perm(odd, even, 0x07060302u); }
typedef __amdgpu_buffer_rsrc_t rsrc_t;
__device__ __forceinline__ rsrc_t mkrs(const void* p, int bytes) { return __builtin_amdgcn_make_buffer_rsrc((void*)p, 0, bytes, RSRC3); }
#define LDSADD(idx, inc) __hip_atomic_fetch_add((LAS unsigned*)(LAS float*)(lds + (lane == 0 ? L_FLAG + (idx) : L_DUMMY + lane)), inc, __ATOMIC_RELAXED, __HIP_MEMORY_SCOPE_WORKGROUP)
#define LGKM0() asm volatile("s_waitcnt lgkmcnt(0)" ::: "memory")
#define CFENCE() asm volatile("" ::: "memory")
#define GRAN2(a, b) u32x4{__float_as_uint(a), epoch, __float_as_uint(b), epoch}
#define LDSWAIT(COND, SLP) for (unsigned spin = 0; (COND) && spin < (SPIN_CAP << 2); ++spin) __builtin_amdgcn_s_sleep(SLP)

__global__ void __launch_bounds__(512, 2) fused(const u16* __restrict__ x, const u16* __restrict__ kc, const u16* __restrict__ vc, const u16* __restrict__ wq,
                                                const u16* __restrict__ wo, const u16* __restrict__ wg, const u16* __restrict__ wu, const u16* __restrict__ wd,
                                                u16* out, unsigned char* ws) {
  __shared__ __attribute__((aligned(16))) float lds[L_WORDS];
  volatile LAS unsigned* flag = (volatile LAS unsigned*)(LAS float*)(lds + L_FLAG);
  volatile LAS unsigned* list = (volatile LAS unsigned*)(LAS float*)(lds + L_LIST);
  const int tid = threadIdx.x, lane = tid & 63, wave8 = __builtin_amdgcn_readfirstlane(tid >> 6), wave = wave8 & 3;
  const int g = blockIdx.x >> 3, s = blockIdx.x & 7, b = g >> 4, kvh = g & 15;
  const unsigned tstart = (unsigned)__builtin_amdgcn_s_memrealtime();
  const int s2 = kvh >> 1;
  const int NP = s == s2 ? NP0 - 7 * XK : NP0 + XK, LBASE = 2 * NP;
  constexpr float SCALE = 0.088388347648318440f, LOG2E = 1.4426950408889634f;
  unsigned long long pc0;
  asm volatile("s_getpc_b64 %0" : "=s"(pc0));
  const u32x4 cpf = __builtin_amdgcn_raw_buffer_load_b128(mkrs((const void*)(pc0 & ~255ull), CODE_B), lane * 16, wave8 == 4 ? g * 1024 : OOB, 0);
  SB();
  asm volatile("" :: "s"(x), "s"(kc), "s"(vc), "s"(wq), "s"(wo), "s"(wg), "s"(wu), "s"(wd), "s"(out), "s"(ws));
  const rsrc_t rws = mkrs(ws, WS_END);
  if (wave8 >= 4) {
    const u16* kb = kc + (size_t)g * (SK * DH); const u16* vb = vc + (size_t)g * (SK * DH);
    const int voffK = ((lane >> 2) & 3) * 256 + (lane >> 4) * 64 + (lane & 3) * 16, voffV = lane * 16;
    const rsrc_t rk = mkrs(kb, GROUP_B), rv = mkrs(vb, GROUP_B);
    const int obase = (s ^ (g >> 2)) << 20, xbase = ((s2 ^ (g >> 2)) << 20) + ((s - (s > s2)) * XK - 7 * XK) * PAIR_B;
    const int tsc = s == s2 ? (128 * 256) / (2 * (NP0 - 7 * XK) + POOL) : (128 * 256) / (2 * (NP0 + XK) + POOL);
#define POFF(p) (((p) < NP0 ? obase : xbase) + (p) * PAIR_B)
#define PACEWAIT(T) do { const int x_ = ((T) * tsc) >> 4; const unsigned due_ = (unsigned)(PACEOFF + ((min(x_, PACETB * 16) * PACE1 + max(x_ - PACETB * 16, 0) * PACE2) >> 8)); \
      for (unsigned spin = 0; (unsigned)__builtin_amdgcn_s_memrealtime() - tstart < due_ && spin < 4096u; ++spin) __builtin_amdgcn_s_sleep(2); } while (0)
    u32x4 R[2 + NEXTRA][8];
#define LD1(d, u, rs, vo, so) R[d][u] = __builtin_amdgcn_raw_buffer_load_b128(rs, (vo) + ((u) & 3) * 1024, (so) + ((u) >> 2) * 4096, AUX_NT)
#define PREFILL(LO, HI) _Pragma("unroll") for (int i = (LO); i < (HI); ++i) { if (i >= PRE_T) VMW(PRE_T - 1); \
      LD1(i >> 3, i & 7, rk, voffK, POFF((i >> 4) * 4 + wave) + ((i >> 3) & 1) * CHUNK_B); SB(); }
    PREFILL(0, 8)
    {
      const u32x4 sent = {~0u, ~0u, ~0u, ~0u}, ninf = {0xff80ff80u, 0xff80ff80u, 0xff80ff80u, 0xff80ff80u};
#pragma unroll
      for (int i = 0; i < 4; ++i) *reinterpret_cast<u32x4*>(lds + L_SC + i * 1024 + wave * 256 + lane * 4) = sent;
      if (wave < 2) *reinterpret_cast<u32x4*>(lds + L_SC + 4096 + wave * 256 + lane * 4) = (wave == 1 && lane >= 48) ? ninf : sent;
      else reinterpret_cast<unsigned*>(lds + L_LIST)[(wave - 2) * 64 + lane] = ~0u;
      LGKM0();
    }
    __builtin_amdgcn_s_barrier();
    SB();
    PREFILL(8, 8 * (2 + NEXTRA))
    asm volatile("" :: "v"(cpf));
    LDSWAIT(flag[1] == 0u, 1);
    CFENCE();
    const unsigned epoch = flag[0];
    const int n = lane & 15;
    const u32x4 qv = *reinterpret_cast<const u32x4*>(reinterpret_cast<const u16*>(lds + L_Q) + (n & 1) * 128 + (4 * (lane >> 4) + (n >> 2)) * 8);
    const u32x4 zero4 = {0u, 0u, 0u, 0u};
    const u32x4 Q0 = (n & 2) ? zero4 : qv, Q1 = (n & 2) ? qv : zero4;
    volatile LAS unsigned* scw = (volatile LAS unsigned*)(LAS float*)(lds + L_SC) + lane;
#define QK(d, rs, vo, so, SC, REFILL) do { \
      f32x4 dd[4]; \
      _Pragma("unroll") for (int p = 0; p < 4; ++p) { dd[p] = mfma(R[d][2 * p], Q0, f32x4{0.f, 0.f, 0.f, 0.f}); dd[p] = mfma(R[d][2 * p + 1], Q1, dd[p]); SB(); \
        if (REFILL) { LD1(d, 2 * p, rs, vo, so); SB(); LD1(d, 2 * p + 1, rs, vo, so); SB(); } } \
      float x0 = dpp<IDENT>(dd[0][0]), x1 = dpp<ROR12>(dd[0][1]), x2 = dpp<ROR8>(dd[0][2]), x3 = dpp<ROR4>(dd[0][3]); \
      x0 = dpp_bank<ROR4, 2>(x0, dd[1][0]); x1 = dpp_bank<IDENT, 2>(x1, dd[1][1]); x2 = dpp_bank<ROR12, 2>(x2, dd[1][2]); x3 = dpp_bank<ROR8, 2>(x3, dd[1][3]); \
      x0 = dpp_bank<ROR8, 4>(x0, dd[2][0]); x1 = dpp_bank<ROR4, 4>(x1, dd[2][1]); x2 = dpp_bank<IDENT, 4>(x2, dd[2][2]); x3 = dpp_bank<ROR12, 4>(x3, dd[2][3]); \
      x0 = dpp_bank<ROR12, 8>(x0, dd[3][0]); x1 = dpp_bank<ROR8, 8>(x1, dd[3][1]); x2 = dpp_bank<ROR4, 8>(x2, dd[3][2]); x3 = dpp_bank<IDENT, 8>(x3, dd[3][3]); \
      SC = rbf((x0 + x1) + (x2 + x3)) * SCALE; } while (0)
#define PV(d, rs, vo, so, SC) do { \
      const float pp = __builtin_amdgcn_exp2f(((SC) - m) * LOG2E); \
      l += pp; \
      const unsigned w_ = pkbf(pp, dpp<0x102>(pp)); \
      const u32x4 PA = {w_, dppu<0x104>(w_), dppu<0x108>(w_), dppu<0x10C>(w_)}; \
      u32x4 bf[8]; \
      SB(); \
      _Pragma("unroll") for (int t_ = 0; t_ < 4; ++t_) { \
        _Pragma("unroll") for (int c = 0; c < 4; ++c) { bf[2 * c][t_] = perm_lo(R[d][2 * t_ + 1][c], R[d][2 * t_][c]); bf[2 * c + 1][t_] = perm_hi(R[d][2 * t_ + 1][c], R[d][2 * t_][c]); } \
        SB(); LD1(d, 2 * t_, rs, vo, so); SB(); LD1(d, 2 * t_ + 1, rs, vo, so); SB(); } \
      _Pragma("unroll") for (int j = 0; j < 8; ++j) acc[j] = mfma(PA, bf[j], acc[j]); \
      SB(); } while (0)
#define RESCALE(SC) do { if (__any((SC) > m)) { \
        float pm = (SC); pm = fmaxf(pm, dpp<XOR2>(pm)); pm = fmaxf(pm, dpp<ROR4>(pm)); pm = fmaxf(pm, dpp<ROR8>(pm)); pm = x16_max(pm); pm = x32_max(pm); \
        const float mn = fmaxf(m, pm), al = __builtin_amdgcn_exp2f((m - mn) * LOG2E); \
        const float al0 = dpp<0x00>(al), al1 = dpp<0x55>(al); \
        l *= al; m = mn; \
        _Pragma("unroll") for (int j = 0; j < 8; ++j) { acc[j][0] *= al0; acc[j][1] *= al1; } } } while (0)
#define POOLITEM(SO) do { const int li = min(t - LBASE, 127); \
        unsigned e = (unsigned)__builtin_amdgcn_readfirstlane((int)list[li]); \
        for (unsigned spin = 0; e == ~0u && spin < (SPIN_CAP << 2); ++spin) e = (unsigned)__builtin_amdgcn_readfirstlane((int)list[li]); \
        SO = e < 1024u ? (int)e * CHUNK_B : GROUP_B; } while (0)
    float m = -3.0e38f;
    int cp = wave, t;
    unsigned lt = LDSADD(8, 1u);
    bool first = true;
    do {
      t = __builtin_amdgcn_readfirstlane((int)lt);
      lt = LDSADD(8, t < LBASE ? 1u : 0u);
      const bool nk = t < NP;
      const int np = nk ? t : (t < LBASE ? t - NP : NROW - 1);
      const rsrc_t rs = mkrs(nk ? kb : vb, GROUP_B);
      const int vo = nk ? voffK : voffV, so = np < NROW - 1 ? POFF(np) : GROUP_B;
      PACEWAIT(t);
      SB();
      { float s0, s1; QK(0, rs, vo, so, s0, true); QK(1, rs, vo, so + CHUNK_B, s1, true);
        const unsigned pk = min(pkbf(s0, s1), 0xfffffffeu); scw[cp * 64] = pk; m = fmaxf(fmaxf(m, bflo(pk)), bfhi(pk)); SB(); }
      if (first) {
#pragma unroll
        for (int d = 2; d < 2 + NEXTRA; d += 2) { float s0, s1; QK(d, rs, 0, 0, s0, false); QK(d + 1, rs, 0, 0, s1, false);
          const unsigned pk = min(pkbf(s0, s1), 0xfffffffeu); scw[((d >> 1) * 4 + wave) * 64] = pk; m = fmaxf(fmaxf(m, bflo(pk)), bfhi(pk)); SB(); }
        first = false;
      }
      cp = np;
    } while (t < NP);
    if (t >= LBASE) lt = (unsigned)t;
    m = fmaxf(m, dpp<XOR2>(m)); m = fmaxf(m, dpp<ROR4>(m)); m = fmaxf(m, dpp<ROR8>(m)); m = x16_max(m); m = x32_max(m);
    f32x4 acc[8];
#pragma unroll
    for (int j = 0; j < 8; ++j) acc[j] = f32x4{0.f, 0.f, 0.f, 0.f};
    float l = 0.f;
    int so;
    do {
      t = __builtin_amdgcn_readfirstlane((int)lt);
      lt = LDSADD(8, 1u);
      unsigned pk = scw[cp * 64];
      const bool nv = t < LBASE;
      so = POFF(t - NP);
      if (!nv) POOLITEM(so);
      const rsrc_t rs0 = mkrs(nv ? vb : kb, GROUP_B);
      const int vo0 = nv ? voffV : voffK, so1 = nv ? so + CHUNK_B : so;
      for (unsigned spin = 0; __any(pk == ~0u) && spin < (SPIN_CAP << 2); ++spin) pk = scw[cp * 64];
      const float sc0 = bflo(pk), sc1 = bfhi(pk), scm = fmaxf(sc0, sc1);
      RESCALE(scm);
      PACEWAIT(t);
      SB();
      PV(0, rs0, vo0, so, sc0);
      PV(1, rv, voffV, so1, sc1);
      cp = t - NP;
    } while (t < LBASE);
    while (so < GROUP_B) {
      t = __builtin_amdgcn_readfirstlane((int)lt);
      lt = LDSADD(8, 1u);
      POOLITEM(so);
      SB();
      float sc;
      QK(0, rk, voffK, so, sc, true);
      sc = rbf(sc);
      RESCALE(sc);
      PV(1, rv, voffV, so, sc);
    }
    l += dpp<XOR2>(l); l += dpp<ROR4>(l); l += dpp<ROR8>(l); l = x16_sum(l); l = x32_sum(l);
    {
      volatile LAS f32x4* aw = (volatile LAS f32x4*)(LAS float*)(lds + L_ACC + wave * 256 + lane * 8);
      if (lane < 16) {
#pragma unroll
        for (int h = 0; h < 2; ++h) {
          aw[h * 32] = f32x4{acc[0][h], acc[1][h], acc[2][h], acc[3][h]};
          aw[h * 32 + 1] = f32x4{acc[4][h], acc[5][h], acc[6][h], acc[7][h]};
        }
        if (lane < 2) *(volatile LAS f32x2*)(LAS float*)(lds + L_ML + (wave * 2 + lane) * 2) = f32x2{m, l};
      }
    }
    const unsigned old = LDSADD(2, 1u);
    CFENCE();
    f32x2 mlw[4]; f32x4 av[4];
#pragma unroll
    for (int w = 0; w < 4; ++w) {
      mlw[w] = *(volatile LAS f32x2*)(LAS float*)(lds + L_ML + w * 4 + (lane >> 5) * 2);
      av[w] = *(volatile LAS f32x4*)(LAS float*)(lds + L_ACC + w * 256 + lane * 4);
    }
    const int po = WS_PART + (g * 8 + s) * 2048 + lane * 16, mo = (lane & 7) == 0 ? WS_ML + g * 1024 + (lane >> 3) * 128 + s * 16 : OOB;
    if (__builtin_amdgcn_readfirstlane(old) == 3u) {
      const float M = fmaxf(fmaxf(mlw[0][0], mlw[1][0]), fmaxf(mlw[2][0], mlw[3][0]));
      float L = 0.f; f32x4 o = {0.f, 0.f, 0.f, 0.f};
#pragma unroll
      for (int w = 0; w < 4; ++w) {
        const float wt = __builtin_amdgcn_exp2f((mlw[w][0] - M) * LOG2E);
        L = fmaf(wt, mlw[w][1], L); o += wt * av[w];
      }
      __builtin_amdgcn_raw_buffer_store_b128(GRAN2(o[0], o[1]), rws, po, 0, AUX_SC1);
      __builtin_amdgcn_raw_buffer_store_b128(GRAN2(o[2], o[3]), rws, po + 1024, 0, AUX_SC1);
      __builtin_amdgcn_raw_buffer_store_b128(GRAN2(M, L), rws, mo, 0, AUX_SC1);
      flag[3] = 1u;
    }
  } else {
    __builtin_amdgcn_s_setprio(3);
    const int hw = wave;
    const rsrc_t rwq = mkrs(wq, D * NQ * 2), rx = mkrs(x, 2 * D * 2);
    const int cb = s & 3, rq = (s >> 2) + 2 * b;
    const int qrow0 = 144 * rq + 36 * hw + (lane >> 3);
    const unsigned epoch = __builtin_amdgcn_raw_buffer_load_b32(rws, WS_EPOCH, 0, AUX_SC1) + 1u;
    SB();
    u32x3 xr[3];
#pragma unroll
    for (int i = 0; i < 3; ++i) xr[i] = __builtin_amdgcn_raw_buffer_load_b96(rx, lane * 36 + 12 * i, 0, 0);
    u32x4 wqr[5];
#pragma unroll
    for (int i = 0; i < 5; ++i) wqr[i] = __builtin_amdgcn_raw_buffer_load_b128(rwq, (i == 4 && lane >= 32) ? OOB : (qrow0 + 8 * i) * (NQ * 2) + (kvh * 256 + cb * 64 + (lane & 7) * 8) * 2, 0, 0);
    if (tid < 16) flag[lane] = lane == 8 ? 2u * (2 + NEXTRA) : 0u;
    LGKM0();
    SB();
    __builtin_amdgcn_s_barrier();
    SB();
    const int gq = lane >> 4, c = lane & 15;
    {
      float v[18];
#pragma unroll
      for (int i = 0; i < 3; ++i)
#pragma unroll
        for (int j = 0; j < 3; ++j) { v[6 * i + 2 * j] = bflo(xr[i][j]); v[6 * i + 2 * j + 1] = bfhi(xr[i][j]); }
      float sm = 0.f;
#pragma unroll
      for (int i = 0; i < 18; ++i) sm += v[i];
      sm += dpp<XOR1>(sm); sm += dpp<XOR2>(sm); sm += dpp<ROR4>(sm); sm += dpp<ROR8>(sm); sm = x16_sum(sm);
      const float mean = sm * (1.f / D);
      float ss = 0.f;
#pragma unroll
      for (int i = 0; i < 18; ++i) { const float d = v[i] - mean; ss = fmaf(d, d, ss); }
      ss += dpp<XOR1>(ss); ss += dpp<XOR2>(ss); ss += dpp<ROR4>(ss); ss += dpp<ROR8>(ss); ss = x16_sum(ss);
      const float mb = rbf(mean), den = rbf(sqrtf(rbf(rbf(ss * (1.f / D)) + 1e-5f))), rden = 1.f / den;
#pragma unroll
      for (int i = 0; i < 18; i += 2) {
        float2 o; o.x = rbf(fdiv(rbf(v[i] - mb), den, rden)); o.y = rbf(fdiv(rbf(v[i + 1] - mb), den, rden));
        *reinterpret_cast<float2*>(lds + L_XN + lane * 18 + i) = o;
      }
      LGKM0();
      float a0[8], a1[8];
#pragma unroll
      for (int j = 0; j < 8; ++j) { a0[j] = 0.f; a1[j] = 0.f; }
#pragma unroll
      for (int i = 0; i < 5; ++i) {
        const int rr = i == 4 ? min(qrow0 + 32, D - 1) : qrow0 + 8 * i;
        const float x0 = lds[L_XN + rr], x1 = lds[L_XN + D + rr];
#pragma unroll
        for (int j = 0; j < 4; ++j) {
          const float lo = bflo(wqr[i][j]), hi = bfhi(wqr[i][j]);
          a0[2 * j] = fmaf(x0, lo, a0[2 * j]); a0[2 * j + 1] = fmaf(x0, hi, a0[2 * j + 1]);
          a1[2 * j] = fmaf(x1, lo, a1[2 * j]); a1[2 * j + 1] = fmaf(x1, hi, a1[2 * j + 1]);
        }
      }
      float r1[8], r2[4];
#pragma unroll
      for (int j = 0; j < 8; ++j) r1[j] = t32_sum(a0[j], a1[j]);
#pragma unroll
      for (int j = 0; j < 4; ++j) { r2[j] = t16_sum(r1[j], r1[4 + j]); r2[j] += dpp<ROR8>(r2[j]); }
      if ((lane & 8) == 0) *reinterpret_cast<f32x4*>(lds + L_QRED + hw * 128 + (lane >> 5) * 64 + (lane & 7) * 8 + ((lane >> 4) & 1) * 4) = f32x4{r2[0], r2[1], r2[2], r2[3]};
      CFENCE();
      (void)LDSADD(6, 1u);
    }
    if (hw) {
      const rsrc_t rwg = mkrs(wg, D * F * 2), rwu = mkrs(wu, D * F * 2), rwd = mkrs(wd, F * D * 2), rwo = mkrs(wo, NQ * D * 2);
      const int colA = (hw - 1) * 192 + 8 * c, colB = (hw - 1) * 192 + 128 + 4 * c;
      const int frow0 = 192 * (hw - 1) + (lane >> 2);
      u32x4 gw[12], uw[12], wa[8], fr[12]; u32x2 wb[8];
#define LDW(rs, row0) _Pragma("unroll") for (int j = 0; j < 8; ++j) { \
      VMW(FFN_T - 1); wa[j] = __builtin_amdgcn_raw_buffer_load_b128(rs, (8 * gq + j) * (D * 2) + colA * 2, (row0) * (D * 2), 0); SB(); \
      VMW(FFN_T - 1); wb[j] = __builtin_amdgcn_raw_buffer_load_b64(rs, (8 * gq + j) * (D * 2) + colB * 2, (row0) * (D * 2), 0); SB(); } \
      _Pragma("unroll") for (int cc = 0; cc < 4; ++cc) { \
        fr[2 * cc] = u32x4{perm_lo(wa[1][cc], wa[0][cc]), perm_lo(wa[3][cc], wa[2][cc]), perm_lo(wa[5][cc], wa[4][cc]), perm_lo(wa[7][cc], wa[6][cc])}; \
        fr[2 * cc + 1] = u32x4{perm_hi(wa[1][cc], wa[0][cc]), perm_hi(wa[3][cc], wa[2][cc]), perm_hi(wa[5][cc], wa[4][cc]), perm_hi(wa[7][cc], wa[6][cc])}; } \
      _Pragma("unroll") for (int cc = 0; cc < 2; ++cc) { \
        fr[8 + 2 * cc] = u32x4{perm_lo(wb[1][cc], wb[0][cc]), perm_lo(wb[3][cc], wb[2][cc]), perm_lo(wb[5][cc], wb[4][cc]), perm_lo(wb[7][cc], wb[6][cc])}; \
        fr[9 + 2 * cc] = u32x4{perm_hi(wb[1][cc], wb[0][cc]), perm_hi(wb[3][cc], wb[2][cc]), perm_hi(wb[5][cc], wb[4][cc]), perm_hi(wb[7][cc], wb[6][cc])}; }
#define YMFMA(AF) do { \
      f32x4 ya[12]; const f32x4 z4 = {0.f, 0.f, 0.f, 0.f}; \
      _Pragma("unroll") for (int j = 0; j < 12; ++j) ya[j] = mfma(AF, fr[j], z4); \
      const float a0_ = (gq & 1) ? ya[2][0] : ya[0][0], a1_ = (gq & 1) ? ya[6][0] : ya[4][0]; e0 += (gq & 2) ? a1_ : a0_; \
      const float b0_ = (gq & 1) ? ya[3][0] : ya[1][0], b1_ = (gq & 1) ? ya[7][0] : ya[5][0]; e1 += (gq & 2) ? b1_ : b0_; \
      e2 += (gq & 1) ? ya[10][0] : ya[8][0]; e3 += (gq & 1) ? ya[11][0] : ya[9][0]; } while (0)
      float e0 = 0.f, e1 = 0.f, e2 = 0.f, e3 = 0.f;
      const int nunit = s == s2 ? 2 : 1;
#pragma unroll 1
      for (int it = 0; it < nunit; ++it) {
        const int jl = s * 8 + (kvh >> 1), jr = jl % 7;
        const int line = it ? s * 8 + 3 : (jl == 63 ? 67 : (jl / 7) * 8 + jr + (jr >= 3));
        const int u0 = line * 64 + (kvh & 1) * 32;
#pragma unroll
        for (int i = 0; i < 12; ++i) {
          VMW(FFN_T - 1); gw[i] = __builtin_amdgcn_raw_buffer_load_b128(rwg, frow0 * (F * 2) + (lane & 3) * 16, 16 * i * (F * 2) + u0 * 2, 0); SB();
          VMW(FFN_T - 1); uw[i] = __builtin_amdgcn_raw_buffer_load_b128(rwu, frow0 * (F * 2) + (lane & 3) * 16, 16 * i * (F * 2) + u0 * 2, 0); SB();
        }
        float ag[8], au[8];
#pragma unroll
        for (int j = 0; j < 8; ++j) { ag[j] = 0.f; au[j] = 0.f; }
#pragma unroll
        for (int i = 0; i < 12; ++i) {
          const float xv = lds[L_XN + b * D + frow0 + 16 * i];
#pragma unroll
          for (int j = 0; j < 4; ++j) {
            ag[2 * j] = fmaf(xv, bflo(gw[i][j]), ag[2 * j]); ag[2 * j + 1] = fmaf(xv, bfhi(gw[i][j]), ag[2 * j + 1]);
            au[2 * j] = fmaf(xv, bflo(uw[i][j]), au[2 * j]); au[2 * j + 1] = fmaf(xv, bfhi(uw[i][j]), au[2 * j + 1]);
          }
        }
        float r1[8], r2[4];
#pragma unroll
        for (int j = 0; j < 8; ++j) r1[j] = t32_sum(ag[j], au[j]);
#pragma unroll
        for (int j = 0; j < 4; ++j) { r2[j] = t16_sum(r1[j], r1[4 + j]); r2[j] += dpp<ROR4>(r2[j]); r2[j] += dpp<ROR8>(r2[j]); }
        if ((lane & 12) == 0) *reinterpret_cast<f32x4*>(lds + L_GU + (it * 3 + hw - 1) * 64 + (lane >> 5) * 32 + (lane & 3) * 8 + ((lane >> 4) & 1) * 4) = f32x4{r2[0], r2[1], r2[2], r2[3]};
        CFENCE();
        (void)LDSADD(4 + it, 1u);
        LDW(rwd, u0)
        LDSWAIT(flag[4 + it] < 3u, 1);
        CFENCE();
        const int cc = lane & 31;
        const float* gu = lds + L_GU + it * 192 + cc;
        const float gt = rbf((gu[0] + gu[64]) + gu[128]), up = rbf((gu[32] + gu[96]) + gu[160]);
        const float y2 = -2.f * 0.7978845608028654f * LOG2E * (gt + 0.044715f * gt * gt * gt);
        const float ge = rbf(gt / (1.f + __builtin_amdgcn_exp2f(y2)));
        reinterpret_cast<__bf16*>(lds + L_H + hw * 16)[cc] = (__bf16)(ge * up);
        LGKM0();
        const u32x4 HA = *reinterpret_cast<const u32x4*>(lds + L_H + hw * 16 + gq * 4);
        YMFMA(HA);
        SB();
      }
      LDW(rwo, kvh * 256 + s * 32)
      const int sp = (lane >> 1) & 7;
      int po = WS_PART + (g * 8 + sp) * 2048 + (s * 8 + gq * 2 + (lane & 1)) * 16, mo = WS_ML + g * 1024 + s * 128 + sp * 16;
      int yoA = WS_Y + ((b * 72 + (hw - 1) * 24 + (lane >> 2)) * 128 + kvh * 8 + s) * 64 + (lane & 3) * 16, yoB = lane < 32 ? yoA + 16 * 8192 : OOB;
#pragma unroll
      for (int j = 0; j < 12; ++j) asm volatile("" : "+v"(fr[j]));
      asm volatile("" : "+v"(e0), "+v"(e1), "+v"(e2), "+v"(e3), "+v"(po), "+v"(mo), "+v"(yoA), "+v"(yoB));
      LDSWAIT((int)flag[8] < LBASE - 6, 32);
      if (hw == 1) {
        const u32x4 w0 = __builtin_amdgcn_raw_buffer_load_b128(rws, po, 0, AUX_SC1), w1 = __builtin_amdgcn_raw_buffer_load_b128(rws, po + 1024, 0, AUX_SC1), w2 = __builtin_amdgcn_raw_buffer_load_b128(rws, mo, 0, AUX_SC1);
        const u32x4 w3 = __builtin_amdgcn_raw_buffer_load_b128(mkrs((const void*)(pc0 & ~255ull), CODE_B + 512), lane * 16, g * 1024, 0);
        asm volatile("" :: "v"(w0), "v"(w1), "v"(w2), "v"(w3));
      } else {
        u32x4 w[4];
#pragma unroll
        for (int r_ = 0; r_ < 4; ++r_) w[r_] = __builtin_amdgcn_raw_buffer_load_b128(rws, lane * 16 + r_ * 1024, blockIdx.x < 144 ? WS_Y + blockIdx.x * 8192 + (hw - 2) * 4096 : OOB, AUX_SC1);
        asm volatile("" :: "v"(w[0]), "v"(w[1]), "v"(w[2]), "v"(w[3]));
      }
      LDSWAIT(flag[3] == 0u, 2);
      u32x4 p0, p1, ml;
      for (unsigned spin = 0;; ++spin) {
        CFENCE();
        p0 = __builtin_amdgcn_raw_buffer_load_b128(rws, po, 0, AUX_SC1);
        p1 = __builtin_amdgcn_raw_buffer_load_b128(rws, po + 1024, 0, AUX_SC1);
        ml = __builtin_amdgcn_raw_buffer_load_b128(rws, mo, 0, AUX_SC1);
        const bool ok = (((p0[1] ^ epoch) | (p0[3] ^ epoch) | (p1[1] ^ epoch)) | ((p1[3] ^ epoch) | (ml[1] ^ epoch) | (ml[3] ^ epoch))) == 0u;
        if (__all(ok) || spin > SPIN_CAP) break;
      }
      const float ms = __uint_as_float(ml[0]);
      float M = ms; M = fmaxf(M, dpp<XOR2>(M)); M = fmaxf(M, dpp<ROR4>(M)); M = fmaxf(M, dpp<ROR8>(M));
      const float wt = __builtin_amdgcn_exp2f((ms - M) * LOG2E);
      float v[5] = {wt * __uint_as_float(p0[0]), wt * __uint_as_float(p0[2]), wt * __uint_as_float(p1[0]), wt * __uint_as_float(p1[2]), wt * __uint_as_float(ml[2])};
#pragma unroll
      for (int j = 0; j < 5; ++j) { v[j] += dpp<XOR2>(v[j]); v[j] += dpp<ROR4>(v[j]); v[j] += dpp<ROR8>(v[j]); }
      const float r0 = __builtin_amdgcn_rcpf(v[4]), inv = fmaf(fmaf(-v[4], r0, 1.f), r0, r0);
      const unsigned o0 = pkbf(v[0] * inv, v[1] * inv), o1 = pkbf(v[2] * inv, v[3] * inv);
      const u32x4 OA = {dppu<0xA0>(o0), dppu<0xA0>(o1), dppu<0xF5>(o0), dppu<0xF5>(o1)};
      YMFMA(OA);
      __builtin_amdgcn_raw_buffer_store_b128(GRAN2(e0, e1), rws, yoA, 0, AUX_SC1);
      __builtin_amdgcn_raw_buffer_store_b128(GRAN2(e2, e3), rws, yoB, 0, AUX_SC1);
      flag[7] = 1u;
    } else {
      for (unsigned spin = 0; flag[6] < 4u && spin < (SPIN_CAP << 2); ++spin) {}
      CFENCE();
      {
        const float2 q0 = *reinterpret_cast<const float2*>(lds + L_QRED + 2 * lane), q1 = *reinterpret_cast<const float2*>(lds + L_QRED + 128 + 2 * lane);
        const float2 q2 = *reinterpret_cast<const float2*>(lds + L_QRED + 256 + 2 * lane), q3 = *reinterpret_cast<const float2*>(lds + L_QRED + 384 + 2 * lane);
        const float qa = (q0.x + q1.x) + (q2.x + q3.x), qb = (q0.y + q1.y) + (q2.y + q3.y);
        __builtin_amdgcn_raw_buffer_store_b128(GRAN2(qa, qb), rws, WS_QP + (((kvh * 2 + (lane >> 5)) * 4 + rq) * 256 + cb * 64 + 2 * (lane & 31)) * 8, 0, AUX_SC1);
      }
#define POLL8(P, base, SLP) do { for (unsigned spin = 0;; ++spin) { CFENCE(); if (SLP) __builtin_amdgcn_s_sleep(SLP); \
        _Pragma("unroll") for (int r_ = 0; r_ < 8; ++r_) P[r_] = __builtin_amdgcn_raw_buffer_load_b128(rws, lane * 16 + (r_ & 3) * 1024, (base) + (r_ >> 2) * 4096, AUX_SC1); \
        unsigned bad_ = 0; _Pragma("unroll") for (int r_ = 0; r_ < 8; ++r_) bad_ |= (P[r_][1] ^ epoch) | (P[r_][3] ^ epoch); \
        if (__all(bad_ == 0u) || spin > SPIN_CAP) break; } } while (0)
      {
        u32x4 p[8];
        __builtin_amdgcn_s_sleep(24);
        POLL8(p, WS_QP + (kvh * 2 + b) * 8192, 8);
#pragma unroll
        for (int hh = 0; hh < 2; ++hh) {
          const float qa = (__uint_as_float(p[hh][0]) + __uint_as_float(p[2 + hh][0])) + (__uint_as_float(p[4 + hh][0]) + __uint_as_float(p[6 + hh][0]));
          const float qb = (__uint_as_float(p[hh][2]) + __uint_as_float(p[2 + hh][2])) + (__uint_as_float(p[4 + hh][2]) + __uint_as_float(p[6 + hh][2]));
          reinterpret_cast<unsigned*>(lds + L_Q)[hh * 64 + lane] = pkbf(qa, qb);
        }
      }
      flag[0] = epoch;
      CFENCE();
      flag[1] = 1u;
      if (s == 0) {
        __builtin_amdgcn_raw_buffer_store_b32(0u, rws, lane == 0 ? WS_CNT + g * 256 : OOB, 0, AUX_SC1);
        asm volatile("s_waitcnt vmcnt(0)" ::: "memory");
        __builtin_amdgcn_raw_buffer_store_b32(epoch, rws, lane == 0 ? WS_CNT + g * 256 + 128 : OOB, 0, AUX_SC1);
      }
      LDSWAIT((int)flag[8] < LBASE - 24, 32);
      for (unsigned spin = 0;; ++spin) {
        CFENCE();
        const unsigned f = __builtin_amdgcn_raw_buffer_load_b32(rws, WS_CNT + g * 256 + 128, 0, AUX_SC1);
        if (__all(f == epoch) || spin > SPIN_CAP) break;
        __builtin_amdgcn_s_sleep(8);
      }
      {
        const rsrc_t rc = mkrs(ws + WS_CNT + g * 256, 4);
        int req = 0;
        for (unsigned spin = 0; spin < (SPIN_CAP << 2); ++spin) {
          if ((int)__builtin_amdgcn_readfirstlane((int)flag[8]) - LBASE <= req) { __builtin_amdgcn_s_sleep(2); continue; }
          const unsigned t0 = (unsigned)__builtin_amdgcn_readfirstlane(__builtin_amdgcn_raw_ptr_buffer_atomic_add_i32(CLAIM, rc, lane == 0 ? 0 : 64, 0, 0));
          const int got = t0 >= (unsigned)NTK ? 0 : min(CLAIM, NTK - (int)t0);
          const unsigned tt = t0 + (unsigned)lane;
          if (lane < got) list[req + lane] = (tt & 7) * 128 + 2 * NP0 + (tt >> 3);
          req += got;
          if (got < CLAIM || (int)t0 + CLAIM >= NTK || req >= NTK) break;
        }
        if (lane >= req) list[lane] = 0x7fffffffu;
        if (lane + 64 >= req) list[lane + 64] = 0x7fffffffu;
      }
      if (blockIdx.x < 144) {
        const int rid = blockIdx.x, rb = rid % 72, rw = rb / 24, rr = rb % 24;
        const int cc = (rr & 3) * 4 + (lane & 3);
        const int od = (rid / 72) * (D / 2) + rw * 96 + (rr < 16 ? 4 * cc + (rr >> 2) : 64 + 2 * cc + ((rr >> 2) & 1));
        const unsigned xres = __builtin_amdgcn_raw_buffer_load_b32(rx, od * 4, 0, 0);
        LDSWAIT(flag[7] == 0u, 2);
        u32x4 y[8];
        POLL8(y, WS_Y + rid * 8192, 0);
        float s0 = 0.f, s1 = 0.f;
#pragma unroll
        for (int i = 0; i < 8; ++i) { s0 += __uint_as_float(y[i][0]); s1 += __uint_as_float(y[i][2]); }
        float st = t32_sum(s0, s1);
        st = x16_sum(st); st += dpp<ROR4>(st); st += dpp<ROR8>(st);
        const auto sw = __builtin_amdgcn_permlane32_swap(__float_as_uint(st), __float_as_uint(st), false, false);
        if (lane < 4) reinterpret_cast<unsigned*>(out)[od] = pkbf(bflo(xres) + __uint_as_float(sw[0]), bfhi(xres) + __uint_as_float(sw[1]));
        if (rid == 0 && lane == 0) __builtin_amdgcn_raw_buffer_store_b32(epoch, rws, WS_EPOCH, 0, AUX_SC1);
      }
    }
  }
}
}

extern "C" void kernel_launch(void* const* d_in, const int* in_sizes, int n_in, void* d_out, int out_size, void* d_ws, size_t ws_size, hipStream_t stream) {
  using fk::u16;
  fk::fused<<<256, 512, 0, stream>>>((const u16*)d_in[0], (const u16*)d_in[1], (const u16*)d_in[2], (const u16*)d_in[3], (const u16*)d_in[4],
                                     (const u16*)d_in[6], (const u16*)d_in[7], (const u16*)d_in[8], (u16*)d_out, (unsigned char*)d_ws);
}
```

```cpp
#include <hip/hip_runtime.h>
#include <hip/hip_bf16.h>
#include <stdint.h>

namespace fk {
typedef unsigned short u16;
typedef unsigned u32x4 __attribute__((ext_vector_type(4)));
typedef unsigned u32x2 __attribute__((ext_vector_type(2)));
typedef unsigned u32x3 __attribute__((ext_vector_type(3)));
typedef float f32x4 __attribute__((ext_vector_type(4)));
typedef float f32x2 __attribute__((ext_vector_type(2)));
typedef __bf16 bf16x8 __attribute__((ext_vector_type(8)));
typedef __bf16 bf16x2 __attribute__((ext_vector_type(2)));
#define LAS __attribute__((address_space(3)))
#define SB() __builtin_amdgcn_sched_barrier(0)

constexpr int D = 576, SK = 32768, DH = 128, F = 4608, NQ = 4096;
constexpr int XK = 2;
constexpr int NEXTRA = 4;
constexpr int PRE_T = 12;
constexpr int FFN_T = 3;
constexpr int CODE_B = 16704;
constexpr int PACE1 = 965, PACE2 = 920, PACETB = 56, PACEOFF = 175;
constexpr int CHUNK_B = 8192, PAIR_B = 16384;
constexpr int NP0 = 64;
constexpr int GROUP_B = SK * DH * 2;
constexpr int NROW = 72;
static_assert(NEXTRA % 2 == 0 && NP0 + XK <= NROW && NP0 - 7 * XK >= 2 * (2 + NEXTRA), "");
constexpr int WS_EPOCH = 0, WS_QP = 4096, WS_PART = WS_QP + 16 * 2 * 4 * 256 * 8, WS_ML = WS_PART + 32 * 8 * 256 * 8,
              WS_Y = WS_ML + 32 * 8 * 8 * 16, WS_END = WS_Y + 144 * 128 * 64;
constexpr int AUX_NT = 2, AUX_SC1 = 16, RSRC3 = 0x00020000, OOB = 0x7f000000;
constexpr unsigned SPIN_CAP = 1u << 18;
constexpr int L_XN = 0;
constexpr int L_QRED = L_XN + 2 * D;
constexpr int L_Q = L_QRED + 4 * 128;
constexpr int L_GU = L_Q + 128;
constexpr int L_H = L_GU + 2 * 3 * 64;
constexpr int L_ACC = L_H + 64;
constexpr int L_ML = L_ACC + 4 * 256;
constexpr int L_FLAG = L_ML + 16;
constexpr int L_DUMMY = L_FLAG + 16;
constexpr int L_SC = L_DUMMY + 64;
constexpr int L_WORDS = L_SC + NROW * 64;
#define VMW(n) __builtin_amdgcn_s_waitcnt(0x0F70 | ((n) & 15) | (((n) >> 4) << 14))

__device__ __forceinline__ float bflo(unsigned v) { return __uint_as_float(v << 16); }
__device__ __forceinline__ float bfhi(unsigned v) { return __uint_as_float(v & 0xffff0000u); }
__device__ __forceinline__ unsigned pkbf(float lo, float hi) { bf16x2 r = {(__bf16)lo, (__bf16)hi}; return __builtin_bit_cast(unsigned, r); }
__device__ __forceinline__ float rbf(float f) { return (float)(__bf16)f; }
__device__ __forceinline__ float fdiv(float a, float d, float r) { const float q = a * r; return fmaf(fmaf(-q, d, a), r, q); }
template <int CTRL> __device__ __forceinline__ float dpp(float x) {
  return __builtin_bit_cast(float, __builtin_amdgcn_mov_dpp(__builtin_bit_cast(int, x), CTRL, 0xf, 0xf, true));
}
template <int CTRL> __device__ __forceinline__ unsigned dppu(unsigned x) { return (unsigned)__builtin_amdgcn_mov_dpp((int)x, CTRL, 0xf, 0xf, true); }
template <int CTRL, int BANK> __device__ __forceinline__ float dpp_bank(float old, float x) {
  return __builtin_bit_cast(float, __builtin_amdgcn_update_dpp(__builtin_bit_cast(int, old), __builtin_bit_cast(int, x), CTRL, 0xf, BANK, false));
}
constexpr int XOR1 = 0xB1, XOR2 = 0x4E, ROR4 = 0x124, ROR8 = 0x128, ROR12 = 0x12C, IDENT = 0xE4;
__device__ __forceinline__ float x16_max(float x) { auto s = __builtin_amdgcn_permlane16_swap(__float_as_uint(x), __float_as_uint(x), false, false); return fmaxf(__uint_as_float(s[0]), __uint_as_float(s[1])); }
__device__ __forceinline__ float x32_max(float x) { auto s = __builtin_amdgcn_permlane32_swap(__float_as_uint(x), __float_as_uint(x), false, false); return fmaxf(__uint_as_float(s[0]), __uint_as_float(s[1])); }
__device__ __forceinline__ float x16_sum(float x) { auto s = __builtin_amdgcn_permlane16_swap(__float_as_uint(x), __float_as_uint(x), false, false); return __uint_as_float(s[0]) + __uint_as_float(s[1]); }
__device__ __forceinline__ float x32_sum(float x) { auto s = __builtin_amdgcn_permlane32_swap(__float_as_uint(x), __float_as_uint(x), false, false); return __uint_as_float(s[0]) + __uint_as_float(s[1]); }
__device__ __forceinline__ float t32_sum(float a, float b) { auto s = __builtin_amdgcn_permlane32_swap(__float_as_uint(a), __float_as_uint(b), false, false); return __uint_as_float(s[0]) + __uint_as_float(s[1]); }
__device__ __forceinline__ float t16_sum(float a, float b) { auto s = __builtin_amdgcn_permlane16_swap(__float_as_uint(a), __float_as_uint(b), false, false); return __uint_as_float(s[0]) + __uint_as_float(s[1]); }
__device__ __forceinline__ f32x4 mfma(u32x4 a, u32x4 b, f32x4 c) {
  return __builtin_amdgcn_mfma_f32_16x16x32_bf16(__builtin_bit_cast(bf16x8, a), __builtin_bit_cast(bf16x8, b), c, 0, 0, 0);
}
__device__ __forceinline__ unsigned perm_lo(unsigned odd, unsigned even) { return __builtin_amdgcn_perm(odd, even, 0x05040100u); }
__device__ __forceinline__ unsigned perm_hi(unsigned odd, unsigned even) { return __builtin_amdgcn_perm(odd, even, 0x07060302u); }
typedef __amdgpu_buffer_rsrc_t rsrc_t;
__device__ __forceinline__ rsrc_t mkrs(const void* p, int bytes) { return __builtin_amdgcn_make_buffer_rsrc((void*)p, 0, bytes, RSRC3); }
#define LDSADD(idx, inc) __hip_atomic_fetch_add((LAS unsigned*)(LAS float*)(lds + (lane == 0 ? L_FLAG + (idx) : L_DUMMY + lane)), inc, __ATOMIC_RELAXED, __HIP_MEMORY_SCOPE_WORKGROUP)
#define LGKM0() asm volatile("s_waitcnt lgkmcnt(0)" ::: "memory")
#define CFENCE() asm volatile("" ::: "memory")
#define GRAN2(a, b) u32x4{__float_as_uint(a), epoch, __float_as_uint(b), epoch}
#define LDSWAIT(COND, SLP) for (unsigned spin = 0; (COND) && spin < (SPIN_CAP << 2); ++spin) __builtin_amdgcn_s_sleep(SLP)

__global__ void __launch_bounds__(512, 2) fused(const u16* __restrict__ x, const u16* __restrict__ kc, const u16* __restrict__ vc, const u16* __restrict__ wq,
                                                const u16* __restrict__ wo, const u16* __restrict__ wg, const u16* __restrict__ wu, const u16* __restrict__ wd,
                                                u16* out, unsigned char* ws) {
  __shared__ __attribute__((aligned(16))) float lds[L_WORDS];
  volatile LAS unsigned* flag = (volatile LAS unsigned*)(LAS float*)(lds + L_FLAG);
  const int tid = threadIdx.x, lane = tid & 63, wave8 = __builtin_amdgcn_readfirstlane(tid >> 6), wave = wave8 & 3;
  const int g = blockIdx.x >> 3, s = blockIdx.x & 7, b = g >> 4, kvh = g & 15;
  const unsigned tstart = (unsigned)__builtin_amdgcn_s_memrealtime();
  const int s2 = kvh >> 1;
  constexpr float SCALE = 0.088388347648318440f, LOG2E = 1.4426950408889634f;
  unsigned long long pc0;
  asm volatile("s_getpc_b64 %0" : "=s"(pc0));
  const u32x4 cpf = __builtin_amdgcn_raw_buffer_load_b128(mkrs((const void*)(pc0 & ~255ull), CODE_B), lane * 16, wave8 == 4 ? g * 1024 : OOB, 0);
  SB();
  asm volatile("" :: "s"(x), "s"(kc), "s"(vc), "s"(wq), "s"(wo), "s"(wg), "s"(wu), "s"(wd), "s"(out), "s"(ws));
  const rsrc_t rws = mkrs(ws, WS_END);
  if (wave8 >= 4) {
    const u16* kb = kc + (size_t)g * (SK * DH); const u16* vb = vc + (size_t)g * (SK * DH);
    const int voffK = ((lane >> 2) & 3) * 256 + (lane >> 4) * 64 + (lane & 3) * 16, voffV = lane * 16;
    const rsrc_t rk = mkrs(kb, GROUP_B), rv = mkrs(vb, GROUP_B);
    const int obase = (s ^ (g >> 2)) << 20, xbase = ((s2 ^ (g >> 2)) << 20) + ((s - (s > s2)) * XK - 7 * XK) * PAIR_B;
    const int NP = s == s2 ? NP0 - 7 * XK : NP0 + XK;
    const int tsc = s == s2 ? (64 * 256) / (NP0 - 7 * XK) : (64 * 256) / (NP0 + XK);
#define POFF(p) (((p) < NP0 ? obase : xbase) + (p) * PAIR_B)
#define PACEWAIT(T) do { const int x_ = ((T) * tsc) >> 4; const unsigned due_ = (unsigned)(PACEOFF + ((min(x_, PACETB * 16) * PACE1 + max(x_ - PACETB * 16, 0) * PACE2) >> 8)); \
      for (unsigned spin = 0; (unsigned)__builtin_amdgcn_s_memrealtime() - tstart < due_ && spin < 4096u; ++spin) __builtin_amdgcn_s_sleep(2); } while (0)
    u32x4 R[2 + NEXTRA][8];
#define LD1(d, u, rs, vo, so) R[d][u] = __builtin_amdgcn_raw_buffer_load_b128(rs, (vo) + ((u) & 3) * 1024, (so) + ((u) >> 2) * 4096, AUX_NT)
#define PREFILL(LO, HI) _Pragma("unroll") for (int i = (LO); i < (HI); ++i) { if (i >= PRE_T) VMW(PRE_T - 1); \
      LD1(i >> 3, i & 7, rk, voffK, POFF((i >> 4) * 4 + wave) + ((i >> 3) & 1) * CHUNK_B); SB(); }
    PREFILL(0, 8)
    {
      const u32x4 sent = {~0u, ~0u, ~0u, ~0u};
#pragma unroll
      for (int i = 0; i < 4; ++i) *reinterpret_cast<u32x4*>(lds + L_SC + i * 1024 + wave * 256 + lane * 4) = sent;
      if (wave < 2) *reinterpret_cast<u32x4*>(lds + L_SC + 4096 + wave * 256 + lane * 4) = sent;
      LGKM0();
    }
    __builtin_amdgcn_s_barrier();
    SB();
    PREFILL(8, 8 * (2 + NEXTRA))
    asm volatile("" :: "v"(cpf));
    LDSWAIT(flag[1] == 0u, 1);
    CFENCE();
    const unsigned epoch = flag[0];
    const int n = lane & 15;
    const u32x4 qv = *reinterpret_cast<const u32x4*>(reinterpret_cast<const u16*>(lds + L_Q) + (n & 1) * 128 + (4 * (lane >> 4) + (n >> 2)) * 8);
    const u32x4 zero4 = {0u, 0u, 0u, 0u};
    const u32x4 Q0 = (n & 2) ? zero4 : qv, Q1 = (n & 2) ? qv : zero4;
    volatile LAS unsigned* scw = (volatile LAS unsigned*)(LAS float*)(lds + L_SC) + lane;
#define QK(d, rs, vo, so, SC, REFILL) do { \
      f32x4 dd[4]; \
      _Pragma("unroll") for (int p = 0; p < 4; ++p) { dd[p] = mfma(R[d][2 * p], Q0, f32x4{0.f, 0.f, 0.f, 0.f}); dd[p] = mfma(R[d][2 * p + 1], Q1, dd[p]); SB(); \
        if (REFILL) { LD1(d, 2 * p, rs, vo, so); SB(); LD1(d, 2 * p + 1, rs, vo, so); SB(); } } \
      float x0 = dpp<IDENT>(dd[0][0]), x1 = dpp<ROR12>(dd[0][1]), x2 = dpp<ROR8>(dd[0][2]), x3 = dpp<ROR4>(dd[0][3]); \
      x0 = dpp_bank<ROR4, 2>(x0, dd[1][0]); x1 = dpp_bank<IDENT, 2>(x1, dd[1][1]); x2 = dpp_bank<ROR12, 2>(x2, dd[1][2]); x3 = dpp_bank<ROR8, 2>(x3, dd[1][3]); \
      x0 = dpp_bank<ROR8, 4>(x0, dd[2][0]); x1 = dpp_bank<ROR4, 4>(x1, dd[2][1]); x2 = dpp_bank<IDENT, 4>(x2, dd[2][2]); x3 = dpp_bank<ROR12, 4>(x3, dd[2][3]); \
      x0 = dpp_bank<ROR12, 8>(x0, dd[3][0]); x1 = dpp_bank<ROR8, 8>(x1, dd[3][1]); x2 = dpp_bank<ROR4, 8>(x2, dd[3][2]); x3 = dpp_bank<IDENT, 8>(x3, dd[3][3]); \
      SC = rbf((x0 + x1) + (x2 + x3)) * SCALE; } while (0)
#define PV(d, rs, vo, so, SC) do { \
      const float pp = __builtin_amdgcn_exp2f(((SC) - m) * LOG2E); \
      l += pp; \
      const unsigned w_ = pkbf(pp, dpp<0x102>(pp)); \
      const u32x4 PA = {w_, dppu<0x104>(w_), dppu<0x108>(w_), dppu<0x10C>(w_)}; \
      u32x4 bf[8]; \
      SB(); \
      _Pragma("unroll") for (int t_ = 0; t_ < 4; ++t_) { \
        _Pragma("unroll") for (int c = 0; c < 4; ++c) { bf[2 * c][t_] = perm_lo(R[d][2 * t_ + 1][c], R[d][2 * t_][c]); bf[2 * c + 1][t_] = perm_hi(R[d][2 * t_ + 1][c], R[d][2 * t_][c]); } \
        SB(); LD1(d, 2 * t_, rs, vo, so); SB(); LD1(d, 2 * t_ + 1, rs, vo, so); SB(); } \
      _Pragma("unroll") for (int j = 0; j < 8; ++j) acc[j] = mfma(PA, bf[j], acc[j]); \
      SB(); } while (0)
#define RESCALE(SC) do { if (__any((SC) > m)) { \
        float pm = (SC); pm = fmaxf(pm, dpp<XOR2>(pm)); pm = fmaxf(pm, dpp<ROR4>(pm)); pm = fmaxf(pm, dpp<ROR8>(pm)); pm = x16_max(pm); pm = x32_max(pm); \
        const float mn = fmaxf(m, pm), al = __builtin_amdgcn_exp2f((m - mn) * LOG2E); \
        const float al0 = dpp<0x00>(al), al1 = dpp<0x55>(al); \
        l *= al; m = mn; \
        _Pragma("unroll") for (int j = 0; j < 8; ++j) { acc[j][0] *= al0; acc[j][1] *= al1; } } } while (0)
    int cp = wave, t;
    unsigned lt = LDSADD(8, 1u);
    bool first = true;
    do {
      t = __builtin_amdgcn_readfirstlane((int)lt);
      lt = LDSADD(8, 1u);
      const bool nk = t < NP;
      const int np = nk ? t : wave;
      const rsrc_t rs = mkrs(nk ? kb : vb, GROUP_B);
      const int vo = nk ? voffK : voffV, so = POFF(np);
      PACEWAIT(nk ? t : NP + wave);
      SB();
      { float s0, s1; QK(0, rs, vo, so, s0, true); QK(1, rs, vo, so + CHUNK_B, s1, true);
        scw[cp * 64] = min(pkbf(s0, s1), 0xfffffffeu); SB(); }
      if (first) {
#pragma unroll
        for (int d = 2; d < 2 + NEXTRA; d += 2) { float s0, s1; QK(d, rs, 0, 0, s0, false); QK(d + 1, rs, 0, 0, s1, false);
          scw[((d >> 1) * 4 + wave) * 64] = min(pkbf(s0, s1), 0xfffffffeu); SB(); }
        first = false;
      }
      cp = np;
    } while (t < NP);
    float m = -3.0e38f;
    f32x4 acc[8];
#pragma unroll
    for (int j = 0; j < 8; ++j) acc[j] = f32x4{0.f, 0.f, 0.f, 0.f};
    float l = 0.f;
    do {
      unsigned pk = scw[cp * 64];
      const int np = cp + 4;
      if (np >= NP - 24 && np < NP - 20) flag[9] = 1u;
      const int so = np < NP ? POFF(np) : GROUP_B;
      for (unsigned spin = 0; __any(pk == ~0u) && spin < (SPIN_CAP << 2); ++spin) pk = scw[cp * 64];
      const float sc0 = bflo(pk), sc1 = bfhi(pk), scm = fmaxf(sc0, sc1);
      RESCALE(scm);
      if (np < NP) PACEWAIT(NP + np);
      SB();
      PV(0, rv, voffV, so, sc0);
      PV(1, rv, voffV, so + CHUNK_B, sc1);
      cp = np;
    } while (cp < NP);
    l += dpp<XOR2>(l); l += dpp<ROR4>(l); l += dpp<ROR8>(l); l = x16_sum(l); l = x32_sum(l);
    {
      volatile LAS f32x4* aw = (volatile LAS f32x4*)(LAS float*)(lds + L_ACC + wave * 256 + lane * 8);
      if (lane < 16) {
#pragma unroll
        for (int h = 0; h < 2; ++h) {
          aw[h * 32] = f32x4{acc[0][h], acc[1][h], acc[2][h], acc[3][h]};
          aw[h * 32 + 1] = f32x4{acc[4][h], acc[5][h], acc[6][h], acc[7][h]};
        }
        if (lane < 2) *(volatile LAS f32x2*)(LAS float*)(lds + L_ML + (wave * 2 + lane) * 2) = f32x2{m, l};
      }
    }
    const unsigned old = LDSADD(2, 1u);
    CFENCE();
    f32x2 mlw[4]; f32x4 av[4];
#pragma unroll
    for (int w = 0; w < 4; ++w) {
      mlw[w] = *(volatile LAS f32x2*)(LAS float*)(lds + L_ML + w * 4 + (lane >> 5) * 2);
      av[w] = *(volatile LAS f32x4*)(LAS float*)(lds + L_ACC + w * 256 + lane * 4);
    }
    const int po = WS_PART + (g * 8 + s) * 2048 + lane * 16, mo = (lane & 7) == 0 ? WS_ML + g * 1024 + (lane >> 3) * 128 + s * 16 : OOB;
    if (__builtin_amdgcn_readfirstlane(old) == 3u) {
      const float M = fmaxf(fmaxf(mlw[0][0], mlw[1][0]), fmaxf(mlw[2][0], mlw[3][0]));
      float L = 0.f; f32x4 o = {0.f, 0.f, 0.f, 0.f};
#pragma unroll
      for (int w = 0; w < 4; ++w) {
        const float wt = __builtin_amdgcn_exp2f((mlw[w][0] - M) * LOG2E);
        L = fmaf(wt, mlw[w][1], L); o += wt * av[w];
      }
      __builtin_amdgcn_raw_buffer_store_b128(GRAN2(o[0], o[1]), rws, po, 0, AUX_SC1);
      __builtin_amdgcn_raw_buffer_store_b128(GRAN2(o[2], o[3]), rws, po + 1024, 0, AUX_SC1);
      __builtin_amdgcn_raw_buffer_store_b128(GRAN2(M, L), rws, mo, 0, AUX_SC1);
      flag[3] = 1u;
    }
  } else {
    __builtin_amdgcn_s_setprio(3);
    const int hw = wave;
    const rsrc_t rwq = mkrs(wq, D * NQ * 2), rx = mkrs(x, 2 * D * 2);
    const int cb = s & 3, rq = (s >> 2) + 2 * b;
    const int qrow0 = 144 * rq + 36 * hw + (lane >> 3);
    const unsigned epoch = __builtin_amdgcn_raw_buffer_load_b32(rws, WS_EPOCH, 0, AUX_SC1) + 1u;
    SB();
    u32x3 xr[3];
#pragma unroll
    for (int i = 0; i < 3; ++i) xr[i] = __builtin_amdgcn_raw_buffer_load_b96(rx, lane * 36 + 12 * i, 0, 0);
    u32x4 wqr[5];
#pragma unroll
    for (int i = 0; i < 5; ++i) wqr[i] = __builtin_amdgcn_raw_buffer_load_b128(rwq, (i == 4 && lane >= 32) ? OOB : (qrow0 + 8 * i) * (NQ * 2) + (kvh * 256 + cb * 64 + (lane & 7) * 8) * 2, 0, 0);
    if (tid < 16) flag[lane] = lane == 8 ? 2u * (2 + NEXTRA) : 0u;
    LGKM0();
    SB();
    __builtin_amdgcn_s_barrier();
    SB();
    const int gq = lane >> 4, c = lane & 15;
    {
      float v[18];
#pragma unroll
      for (int i = 0; i < 3; ++i)
#pragma unroll
        for (int j = 0; j < 3; ++j) { v[6 * i + 2 * j] = bflo(xr[i][j]); v[6 * i + 2 * j + 1] = bfhi(xr[i][j]); }
      float sm = 0.f;
#pragma unroll
      for (int i = 0; i < 18; ++i) sm += v[i];
      sm += dpp<XOR1>(sm); sm += dpp<XOR2>(sm); sm += dpp<ROR4>(sm); sm += dpp<ROR8>(sm); sm = x16_sum(sm);
      const float mean = sm * (1.f / D);
      float ss = 0.f;
#pragma unroll
      for (int i = 0; i < 18; ++i) { const float d = v[i] - mean; ss = fmaf(d, d, ss); }
      ss += dpp<XOR1>(ss); ss += dpp<XOR2>(ss); ss += dpp<ROR4>(ss); ss += dpp<ROR8>(ss); ss = x16_sum(ss);
      const float mb = rbf(mean), den = rbf(sqrtf(rbf(rbf(ss * (1.f / D)) + 1e-5f))), rden = 1.f / den;
#pragma unroll
      for (int i = 0; i < 18; i += 2) {
        float2 o; o.x = rbf(fdiv(rbf(v[i] - mb), den, rden)); o.y = rbf(fdiv(rbf(v[i + 1] - mb), den, rden));
        *reinterpret_cast<float2*>(lds + L_XN + lane * 18 + i) = o;
      }
      LGKM0();
      float a0[8], a1[8];
#pragma unroll
      for (int j = 0; j < 8; ++j) { a0[j] = 0.f; a1[j] = 0.f; }
#pragma unroll
      for (int i = 0; i < 5; ++i) {
        const int rr = i == 4 ? min(qrow0 + 32, D - 1) : qrow0 + 8 * i;
        const float x0 = lds[L_XN + rr], x1 = lds[L_XN + D + rr];
#pragma unroll
        for (int j = 0; j < 4; ++j) {
          const float lo = bflo(wqr[i][j]), hi = bfhi(wqr[i][j]);
          a0[2 * j] = fmaf(x0, lo, a0[2 * j]); a0[2 * j + 1] = fmaf(x0, hi, a0[2 * j + 1]);
          a1[2 * j] = fmaf(x1, lo, a1[2 * j]); a1[2 * j + 1] = fmaf(x1, hi, a1[2 * j + 1]);
        }
      }
      float r1[8], r2[4];
#pragma unroll
      for (int j = 0; j < 8; ++j) r1[j] = t32_sum(a0[j], a1[j]);
#pragma unroll
      for (int j = 0; j < 4; ++j) { r2[j] = t16_sum(r1[j], r1[4 + j]); r2[j] += dpp<ROR8>(r2[j]); }
      if ((lane & 8) == 0) *reinterpret_cast<f32x4*>(lds + L_QRED + hw * 128 + (lane >> 5) * 64 + (lane & 7) * 8 + ((lane >> 4) & 1) * 4) = f32x4{r2[0], r2[1], r2[2], r2[3]};
      CFENCE();
      (void)LDSADD(6, 1u);
    }
    if (hw) {
      const rsrc_t rwg = mkrs(wg, D * F * 2), rwu = mkrs(wu, D * F * 2), rwd = mkrs(wd, F * D * 2), rwo = mkrs(wo, NQ * D * 2);
      const int colA = (hw - 1) * 192 + 8 * c, colB = (hw - 1) * 192 + 128 + 4 * c;
      const int frow0 = 192 * (hw - 1) + (lane >> 2);
      u32x4 gw[12], uw[12], wa[8], fr[12]; u32x2 wb[8];
#define LDW(rs, row0) _Pragma("unroll") for (int j = 0; j < 8; ++j) { \
      VMW(FFN_T - 1); wa[j] = __builtin_amdgcn_raw_buffer_load_b128(rs, (8 * gq + j) * (D * 2) + colA * 2, (row0) * (D * 2), 0); SB(); \
      VMW(FFN_T - 1); wb[j] = __builtin_amdgcn_raw_buffer_load_b64(rs, (8 * gq + j) * (D * 2) + colB * 2, (row0) * (D * 2), 0); SB(); } \
      _Pragma("unroll") for (int cc = 0; cc < 4; ++cc) { \
        fr[2 * cc] = u32x4{perm_lo(wa[1][cc], wa[0][cc]), perm_lo(wa[3][cc], wa[2][cc]), perm_lo(wa[5][cc], wa[4][cc]), perm_lo(wa[7][cc], wa[6][cc])}; \
        fr[2 * cc + 1] = u32x4{perm_hi(wa[1][cc], wa[0][cc]), perm_hi(wa[3][cc], wa[2][cc]), perm_hi(wa[5][cc], wa[4][cc]), perm_hi(wa[7][cc], wa[6][cc])}; } \
      _Pragma("unroll") for (int cc = 0; cc < 2; ++cc) { \
        fr[8 + 2 * cc] = u32x4{perm_lo(wb[1][cc], wb[0][cc]), perm_lo(wb[3][cc], wb[2][cc]), perm_lo(wb[5][cc], wb[4][cc]), perm_lo(wb[7][cc], wb[6][cc])}; \
        fr[9 + 2 * cc] = u32x4{perm_hi(wb[1][cc], wb[0][cc]), perm_hi(wb[3][cc], wb[2][cc]), perm_hi(wb[5][cc], wb[4][cc]), perm_hi(wb[7][cc], wb[6][cc])}; }
#define YMFMA(AF) do { \
      f32x4 ya[12]; const f32x4 z4 = {0.f, 0.f, 0.f, 0.f}; \
      _Pragma("unroll") for (int j = 0; j < 12; ++j) ya[j] = mfma(AF, fr[j], z4); \
      const float a0_ = (gq & 1) ? ya[2][0] : ya[0][0], a1_ = (gq & 1) ? ya[6][0] : ya[4][0]; e0 += (gq & 2) ? a1_ : a0_; \
      const float b0_ = (gq & 1) ? ya[3][0] : ya[1][0], b1_ = (gq & 1) ? ya[7][0] : ya[5][0]; e1 += (gq & 2) ? b1_ : b0_; \
      e2 += (gq & 1) ? ya[10][0] : ya[8][0]; e3 += (gq & 1) ? ya[11][0] : ya[9][0]; } while (0)
      float e0 = 0.f, e1 = 0.f, e2 = 0.f, e3 = 0.f;
      const int nunit = s == s2 ? 2 : 1;
#pragma unroll 1
      for (int it = 0; it < nunit; ++it) {
        const int jl = s * 8 + (kvh >> 1), jr = jl % 7;
        const int line = it ? s * 8 + 3 : (jl == 63 ? 67 : (jl / 7) * 8 + jr + (jr >= 3));
        const int u0 = line * 64 + (kvh & 1) * 32;
#pragma unroll
        for (int i = 0; i < 12; ++i) {
          VMW(FFN_T - 1); gw[i] = __builtin_amdgcn_raw_buffer_load_b128(rwg, frow0 * (F * 2) + (lane & 3) * 16, 16 * i * (F * 2) + u0 * 2, 0); SB();
          VMW(FFN_T - 1); uw[i] = __builtin_amdgcn_raw_buffer_load_b128(rwu, frow0 * (F * 2) + (lane & 3) * 16, 16 * i * (F * 2) + u0 * 2, 0); SB();
        }
        float ag[8], au[8];
#pragma unroll
        for (int j = 0; j < 8; ++j) { ag[j] = 0.f; au[j] = 0.f; }
#pragma unroll
        for (int i = 0; i < 12; ++i) {
          const float xv = lds[L_XN + b * D + frow0 + 16 * i];
#pragma unroll
          for (int j = 0; j < 4; ++j) {
            ag[2 * j] = fmaf(xv, bflo(gw[i][j]), ag[2 * j]); ag[2 * j + 1] = fmaf(xv, bfhi(gw[i][j]), ag[2 * j + 1]);
            au[2 * j] = fmaf(xv, bflo(uw[i][j]), au[2 * j]); au[2 * j + 1] = fmaf(xv, bfhi(uw[i][j]), au[2 * j + 1]);
          }
        }
        float r1[8], r2[4];
#pragma unroll
        for (int j = 0; j < 8; ++j) r1[j] = t32_sum(ag[j], au[j]);
#pragma unroll
        for (int j = 0; j < 4; ++j) { r2[j] = t16_sum(r1[j], r1[4 + j]); r2[j] += dpp<ROR4>(r2[j]); r2[j] += dpp<ROR8>(r2[j]); }
        if ((lane & 12) == 0) *reinterpret_cast<f32x4*>(lds + L_GU + (it * 3 + hw - 1) * 64 + (lane >> 5) * 32 + (lane & 3) * 8 + ((lane >> 4) & 1) * 4) = f32x4{r2[0], r2[1], r2[2], r2[3]};
        CFENCE();
        (void)LDSADD(4 + it, 1u);
        LDW(rwd, u0)
        LDSWAIT(flag[4 + it] < 3u, 1);
        CFENCE();
        const int cc = lane & 31;
        const float* gu = lds + L_GU + it * 192 + cc;
        const float gt = rbf((gu[0] + gu[64]) + gu[128]), up = rbf((gu[32] + gu[96]) + gu[160]);
        const float y2 = -2.f * 0.7978845608028654f * LOG2E * (gt + 0.044715f * gt * gt * gt);
        const float ge = rbf(gt / (1.f + __builtin_amdgcn_exp2f(y2)));
        reinterpret_cast<__bf16*>(lds + L_H + hw * 16)[cc] = (__bf16)(ge * up);
        LGKM0();
        const u32x4 HA = *reinterpret_cast<const u32x4*>(lds + L_H + hw * 16 + gq * 4);
        YMFMA(HA);
        SB();
      }
      LDW(rwo, kvh * 256 + s * 32)
      const int sp = (lane >> 1) & 7;
      int po = WS_PART + (g * 8 + sp) * 2048 + (s * 8 + gq * 2 + (lane & 1)) * 16, mo = WS_ML + g * 1024 + s * 128 + sp * 16;
      int yoA = WS_Y + ((b * 72 + (hw - 1) * 24 + (lane >> 2)) * 128 + kvh * 8 + s) * 64 + (lane & 3) * 16, yoB = lane < 32 ? yoA + 16 * 8192 : OOB;
#pragma unroll
      for (int j = 0; j < 12; ++j) asm volatile("" : "+v"(fr[j]));
      asm volatile("" : "+v"(e0), "+v"(e1), "+v"(e2), "+v"(e3), "+v"(po), "+v"(mo), "+v"(yoA), "+v"(yoB));
      LDSWAIT(flag[9] == 0u, 32);
      if (hw == 1) {
        const u32x4 w0 = __builtin_amdgcn_raw_buffer_load_b128(rws, po, 0, AUX_SC1), w1 = __builtin_amdgcn_raw_buffer_load_b128(rws, po + 1024, 0, AUX_SC1), w2 = __builtin_amdgcn_raw_buffer_load_b128(rws, mo, 0, AUX_SC1);
        const u32x4 w3 = __builtin_amdgcn_raw_buffer_load_b128(mkrs((const void*)(pc0 & ~255ull), CODE_B), lane * 16, g * 1024, 0);
        asm volatile("" :: "v"(w0), "v"(w1), "v"(w2), "v"(w3));
      } else {
        u32x4 w[4];
#pragma unroll
        for (int r_ = 0; r_ < 4; ++r_) w[r_] = __builtin_amdgcn_raw_buffer_load_b128(rws, lane * 16 + r_ * 1024, blockIdx.x < 144 ? WS_Y + blockIdx.x * 8192 + (hw - 2) * 4096 : OOB, AUX_SC1);
        asm volatile("" :: "v"(w[0]), "v"(w[1]), "v"(w[2]), "v"(w[3]));
      }
      LDSWAIT(flag[3] == 0u, 2);
      __builtin_amdgcn_s_sleep(10);
      u32x4 p0, p1, ml;
      for (unsigned spin = 0;; ++spin) {
        CFENCE();
        p0 = __builtin_amdgcn_raw_buffer_load_b128(rws, po, 0, AUX_SC1);
        p1 = __builtin_amdgcn_raw_buffer_load_b128(rws, po + 1024, 0, AUX_SC1);
        ml = __builtin_amdgcn_raw_buffer_load_b128(rws, mo, 0, AUX_SC1);
        const bool ok = (((p0[1] ^ epoch) | (p0[3] ^ epoch) | (p1[1] ^ epoch)) | ((p1[3] ^ epoch) | (ml[1] ^ epoch) | (ml[3] ^ epoch))) == 0u;
        if (__all(ok) || spin > SPIN_CAP) break;
      }
      const float ms = __uint_as_float(ml[0]);
      float M = ms; M = fmaxf(M, dpp<XOR2>(M)); M = fmaxf(M, dpp<ROR4>(M)); M = fmaxf(M, dpp<ROR8>(M));
      const float wt = __builtin_amdgcn_exp2f((ms - M) * LOG2E);
      float v[5] = {wt * __uint_as_float(p0[0]), wt * __uint_as_float(p0[2]), wt * __uint_as_float(p1[0]), wt * __uint_as_float(p1[2]), wt * __uint_as_float(ml[2])};
#pragma unroll
      for (int j = 0; j < 5; ++j) { v[j] += dpp<XOR2>(v[j]); v[j] += dpp<ROR4>(v[j]); v[j] += dpp<ROR8>(v[j]); }
      const float r0 = __builtin_amdgcn_rcpf(v[4]), inv = fmaf(fmaf(-v[4], r0, 1.f), r0, r0);
      const unsigned o0 = pkbf(v[0] * inv, v[1] * inv), o1 = pkbf(v[2] * inv, v[3] * inv);
      const u32x4 OA = {dppu<0xA0>(o0), dppu<0xA0>(o1), dppu<0xF5>(o0), dppu<0xF5>(o1)};
      YMFMA(OA);
      __builtin_amdgcn_raw_buffer_store_b128(GRAN2(e0, e1), rws, yoA, 0, AUX_SC1);
      __builtin_amdgcn_raw_buffer_store_b128(GRAN2(e2, e3), rws, yoB, 0, AUX_SC1);
      flag[7] = 1u;
    } else {
      for (unsigned spin = 0; flag[6] < 4u && spin < (SPIN_CAP << 2); ++spin) {}
      CFENCE();
      {
        const float2 q0 = *reinterpret_cast<const float2*>(lds + L_QRED + 2 * lane), q1 = *reinterpret_cast<const float2*>(lds + L_QRED + 128 + 2 * lane);
        const float2 q2 = *reinterpret_cast<const float2*>(lds + L_QRED + 256 + 2 * lane), q3 = *reinterpret_cast<const float2*>(lds + L_QRED + 384 + 2 * lane);
        const float qa = (q0.x + q1.x) + (q2.x + q3.x), qb = (q0.y + q1.y) + (q2.y + q3.y);
        __builtin_amdgcn_raw_buffer_store_b128(GRAN2(qa, qb), rws, WS_QP + (((kvh * 2 + (lane >> 5)) * 4 + rq) * 256 + cb * 64 + 2 * (lane & 31)) * 8, 0, AUX_SC1);
      }
#define POLL8(P, base, SLP) do { for (unsigned spin = 0;; ++spin) { CFENCE(); if (SLP) __builtin_amdgcn_s_sleep(SLP); \
        _Pragma("unroll") for (int r_ = 0; r_ < 8; ++r_) P[r_] = __builtin_amdgcn_raw_buffer_load_b128(rws, lane * 16 + (r_ & 3) * 1024, (base) + (r_ >> 2) * 4096, AUX_SC1); \
        unsigned bad_ = 0; _Pragma("unroll") for (int r_ = 0; r_ < 8; ++r_) bad_ |= (P[r_][1] ^ epoch) | (P[r_][3] ^ epoch); \
        if (__all(bad_ == 0u) || spin > SPIN_CAP) break; } } while (0)
      {
        u32x4 p[8];
        __builtin_amdgcn_s_sleep(24);
        POLL8(p, WS_QP + (kvh * 2 + b) * 8192, 8);
#pragma unroll
        for (int hh = 0; hh < 2; ++hh) {
          const float qa = (__uint_as_float(p[hh][0]) + __uint_as_float(p[2 + hh][0])) + (__uint_as_float(p[4 + hh][0]) + __uint_as_float(p[6 + hh][0]));
          const float qb = (__uint_as_float(p[hh][2]) + __uint_as_float(p[2 + hh][2])) + (__uint_as_float(p[4 + hh][2]) + __uint_as_float(p[6 + hh][2]));
          reinterpret_cast<unsigned*>(lds + L_Q)[hh * 64 + lane] = pkbf(qa, qb);
        }
      }
      flag[0] = epoch;
      CFENCE();
      flag[1] = 1u;
      if (blockIdx.x < 144) {
        const int rid = blockIdx.x, rb = rid % 72, rw = rb / 24, rr = rb % 24;
        const int cc = (rr & 3) * 4 + (lane & 3);
        const int od = (rid / 72) * (D / 2) + rw * 96 + (rr < 16 ? 4 * cc + (rr >> 2) : 64 + 2 * cc + ((rr >> 2) & 1));
        const unsigned xres = __builtin_amdgcn_raw_buffer_load_b32(rx, od * 4, 0, 0);
        LDSWAIT(flag[9] == 0u, 32);
        LDSWAIT(flag[7] == 0u, 2);
        u32x4 y[8];
        __builtin_amdgcn_s_sleep(24);
        POLL8(y, WS_Y + rid * 8192, 2);
        float s0 = 0.f, s1 = 0.f;
#pragma unroll
        for (int i = 0; i < 8; ++i) { s0 += __uint_as_float(y[i][0]); s1 += __uint_as_float(y[i][2]); }
        float st = t32_sum(s0, s1);
        st = x16_sum(st); st += dpp<ROR4>(st); st += dpp<ROR8>(st);
        const auto sw = __builtin_amdgcn_permlane32_swap(__float_as_uint(st), __float_as_uint(st), false, false);
        if (lane < 4) reinterpret_cast<unsigned*>(out)[od] = pkbf(bflo(xres) + __uint_as_float(sw[0]), bfhi(xres) + __uint_as_float(sw[1]));
        if (rid == 0 && lane == 0) __builtin_amdgcn_raw_buffer_store_b32(epoch, rws, WS_EPOCH, 0, AUX_SC1);
      }
    }
  }
}
}

extern "C" void kernel_launch(void* const* d_in, const int* in_sizes, int n_in, void* d_out, int out_size, void* d_ws, size_t ws_size, hipStream_t stream) {
  using fk::u16;
  fk::fused<<<256, 512, 0, stream>>>((const u16*)d_in[0], (const u16*)d_in[1], (const u16*)d_in[2], (const u16*)d_in[3], (const u16*)d_in[4],
                                     (const u16*)d_in[6], (const u16*)d_in[7], (const u16*)d_in[8], (u16*)d_out, (unsigned char*)d_ws);
}
```
